```python
import torch

def _softmax(x, axis=-1):
    return torch.softmax(x, dim=axis)
def _gelu(x):
    return torch.nn.functional.gelu(x, approximate="tanh")
def _silu(x):
    return torch.nn.functional.silu(x)
def _sigmoid(x):
    return torch.sigmoid(x)
def _layernorm(x, axis=-1, eps=1e-5):
    m = x.mean(dim=axis, keepdim=True)
    v = x.var(dim=axis, keepdim=True, unbiased=False)
    return (x - m) / torch.sqrt(v + eps)
def _scatter_add(tgt, idx, src, axis=0):
    return tgt.clone().index_add_(axis, idx, src)
def _segment_sum(src, seg, n):
    out = torch.zeros((n,) + src.shape[1:], dtype=src.dtype)
    return out.index_add_(0, seg, src)
def _pad(x, pw):
    flat = tuple(v for pair in reversed(pw) for v in pair)
    return torch.nn.functional.pad(x, flat)

kernel_name = 'parallel_sum__spectral_fnet__dense_activation_relu__prompt'

def setup_inputs():

    _w = (lambda a: (lambda t: t.to(torch.bfloat16) if t.is_floating_point() else t)(torch.as_tensor(a)))
    def _A_setup():
        B, S, D = 1, 2048, 512
        ns = torch.arange(S)[:, None] * torch.arange(S)[None, :]
        nd = torch.arange(D)[:, None] * torch.arange(D)[None, :]
        Ws_re = torch.cos(-2.0 * torch.pi * ns / S).to(float)
        Ws_im = torch.sin(-2.0 * torch.pi * ns / S).to(float)
        Wd_re = torch.cos(-2.0 * torch.pi * nd / D).to(float)
        Wd_im = torch.sin(-2.0 * torch.pi * nd / D).to(float)
        return {
            "x": _w((lambda *s: torch.randn(*s, dtype=torch.bfloat16) * 0.05)(B, S, D)),
            "dft_seq_re": _w(Ws_re),
            "dft_seq_im": _w(Ws_im),
            "dft_hid_re": _w(Wd_re),
            "dft_hid_im": _w(Wd_im),
        }
    _A_in = _A_setup()

    def _B_setup():
        B, S, D, Dff = 1, 2048, 512, 1792
        return {
            "x": _w((lambda *s: torch.randn(*s, dtype=torch.bfloat16) * 0.05)(B, S, D)),
            "w1": _w((lambda *s: torch.randn(*s, dtype=torch.bfloat16) * 0.4)(D, Dff)),
            "w2": _w((lambda *s: torch.randn(*s, dtype=torch.bfloat16) * 0.4)(Dff, D)),
        }
    _B_in = _B_setup()

    return {
        "A_x": _A_in["x"],
        "A_dft_seq_re": _A_in["dft_seq_re"],
        "A_dft_seq_im": _A_in["dft_seq_im"],
        "A_dft_hid_re": _A_in["dft_hid_re"],
        "A_dft_hid_im": _A_in["dft_hid_im"],
        "B_x": _B_in["x"],
        "B_w1": _B_in["w1"],
        "B_w2": _B_in["w2"],
    }

def reference(A_x, A_dft_seq_re, A_dft_seq_im, A_dft_hid_re, A_dft_hid_im, B_x, B_w1, B_w2):
    def _A_ref(x, dft_seq_re, dft_seq_im, dft_hid_re, dft_hid_im):
        a = torch.einsum("bsd,ed->bse", x, dft_hid_re)
        b = torch.einsum("bsd,ed->bse", x, dft_hid_im)
        y = torch.einsum("ts,bse->bte", dft_seq_re, a) - torch.einsum("ts,bse->bte", dft_seq_im, b)
        return y

    def _B_ref(x, w1, w2):
        h = torch.einsum("bsd,df->bsf", x, w1)
        zero = torch.zeros_like(h)
        h = torch.maximum(h, zero)
        return torch.einsum("bsf,fd->bsd", h, w2)

    x = A_x
    xn = _layernorm(x, axis=-1)
    return x + _A_ref(xn, A_dft_seq_re, A_dft_seq_im, A_dft_hid_re, A_dft_hid_im) + _B_ref(xn, B_w1, B_w2)
```

```cpp
#include <hip/hip_runtime.h>
#include <stdint.h>

typedef short s16x8 __attribute__((ext_vector_type(8)));
typedef short s16x4 __attribute__((ext_vector_type(4)));
typedef __bf16 bf16x2 __attribute__((ext_vector_type(2)));
typedef float f32x4 __attribute__((ext_vector_type(4)));
typedef unsigned u32x4 __attribute__((ext_vector_type(4)));
typedef unsigned u32x2 __attribute__((ext_vector_type(2)));
typedef unsigned short u16;
#define LAS __attribute__((address_space(3)))
#define GAS __attribute__((address_space(1)))

constexpr int S_ = 2048, D_ = 512, F_ = 1792;
constexpr int HLD = 2048;

#define WAIT_V(n) asm volatile("s_waitcnt vmcnt(%0)" ::"n"(n) : "memory")
#define WAIT_L0() asm volatile("s_waitcnt lgkmcnt(0)" ::: "memory")
#define BARRIER() do { WAIT_L0(); __builtin_amdgcn_s_barrier(); asm volatile("" ::: "memory"); } while (0)

__device__ __forceinline__ float bf2f(unsigned h) { return __uint_as_float(h << 16); }
__device__ __forceinline__ float rbf(float f) {
  __bf16 b = (__bf16)f;
  return (float)b;
}
__device__ __forceinline__ unsigned pack2(float lo, float hi) {
  bf16x2 v;
  v[0] = (__bf16)lo;
  v[1] = (__bf16)hi;
  return __builtin_bit_cast(unsigned, v);
}
__device__ __forceinline__ void glds16(const void* g, char* l) {
  __builtin_amdgcn_global_load_lds((const GAS unsigned*)g, (LAS unsigned*)l, 16, 0, 0);
}
__device__ __forceinline__ s16x8 lds_b128(const char* p) { return *(const LAS s16x8*)p; }
__device__ __forceinline__ s16x4 lds_tr(const char* p) {
  return __builtin_amdgcn_ds_read_tr16_b64_v4i16((LAS s16x4*)p);
}
__device__ __forceinline__ s16x8 cat(s16x4 a, s16x4 b) {
  return __builtin_shufflevector(a, b, 0, 1, 2, 3, 4, 5, 6, 7);
}
__device__ __forceinline__ f32x4 mfma16(s16x8 a, s16x8 b, f32x4 c) {
  return __builtin_amdgcn_mfma_f32_16x16x32_bf16(a, b, c, 0, 0, 0);
}
__device__ __forceinline__ float dot2(unsigned a, unsigned b, float c) {
  return __builtin_amdgcn_fdot2_f32_bf16(__builtin_bit_cast(bf16x2, a), __builtin_bit_cast(bf16x2, b), c, false);
}

constexpr int K1_STAGE = 40960;
constexpr int K1_LDS = 4 * K1_STAGE;

template <bool BT>
__device__ __forceinline__ void k1_body(char* smem, const u16* __restrict__ X, const u16* __restrict__ Hr,
                                        const u16* __restrict__ Hi, const u16* __restrict__ W1,
                                        u16* __restrict__ AB, u16* __restrict__ H, int m0, int n0) {
  const int tid = threadIdx.x, lane = tid & 63;
  const int w = __builtin_amdgcn_readfirstlane(tid >> 6);
  const int wm = w >> 1, wn = w & 1;
  const char* aptr[4];
#pragma unroll
  for (int i = 0; i < 4; ++i) {
    int row = (w * 4 + i) * 8 + (lane >> 3);
    int ch = (lane & 7) ^ (row & 7);
    aptr[i] = (const char*)(X + (size_t)(m0 + row) * D_) + ch * 16;
  }
  const char* bptr[6];
#pragma unroll
  for (int i = 0; i < 6; ++i) {
    int u = (w * 6 + i) * 64 + lane;
    if constexpr (BT) {
      int row = u >> 3;
      int ch = (u & 7) ^ (row & 7);
      int n = n0 + row;
      n = n > 1023 ? 1023 : n;
      const u16* src = n < 512 ? Hr + (size_t)n * D_ : Hi + (size_t)(n - 512) * D_;
      bptr[i] = (const char*)src + ch * 16;
    } else {
      int k = u / 24, pc = u - k * 24;
      int f = ((k >> 1) & 1) | (((k >> 3) & 1) << 1);
      int lc = pc ^ (f << 1);
      int col = n0 + lc * 8;
      col = col > F_ - 8 ? F_ - 8 : col;
      bptr[i] = (const char*)(W1 + (size_t)k * F_ + col);
    }
  }
  constexpr int BSTEP = BT ? 128 : 64 * F_ * 2;
  auto stage = [&](int s) {
    char* base = smem + (s & 3) * K1_STAGE;
#pragma unroll
    for (int i = 0; i < 4; ++i) glds16(aptr[i] + s * 128, base + (w * 4 + i) * 1024);
#pragma unroll
    for (int i = 0; i < 6; ++i) glds16(bptr[i] + (size_t)s * BSTEP, base + 16384 + (w * 6 + i) * 1024);
  };
  stage(0); stage(1); stage(2); stage(3);

  const int g = lane >> 4, r16 = lane & 15;
  int aoff[4];
#pragma unroll
  for (int mi = 0; mi < 4; ++mi) {
    int row = wm * 64 + (((mi + 2 * wn) & 3) * 16) + r16;
    aoff[mi] = row * 128 + ((g ^ (row & 7)) * 16);
  }
  int boff[6];
#pragma unroll
  for (int ni = 0; ni < 6; ++ni) {
    int pb = ni + 3 * wm;
    pb = pb >= 6 ? pb - 6 : pb;
    int nb = wn * 6 + pb;
    if constexpr (BT) {
      int row = nb * 16 + r16;
      boff[ni] = 16384 + row * 128 + ((g ^ (row & 7)) * 16);
    } else {
      int q = r16 >> 2, p = lane & 3;
      int k = 8 * g + q;
      int f = ((k >> 1) & 1) | (((k >> 3) & 1) << 1);
      int lc = nb * 2 + (p >> 1);
      boff[ni] = 16384 + k * 384 + ((lc ^ (f << 1)) * 16) + 8 * (p & 1);
    }
  }

  f32x4 acc[6][4];
#pragma unroll
  for (int ni = 0; ni < 6; ++ni)
#pragma unroll
    for (int mi = 0; mi < 4; ++mi) acc[ni][mi] = (f32x4){0.f, 0.f, 0.f, 0.f};
  float rs[2] = {0.f, 0.f}, rq[2] = {0.f, 0.f}, cs[3] = {0.f, 0.f, 0.f};

#pragma unroll
  for (int s = 0; s < 8; ++s) {
    if (s == 0) WAIT_V(30);
    else if (s <= 5) WAIT_V(20);
    else if (s == 6) WAIT_V(10);
    else WAIT_V(0);
    BARRIER();
    if (s >= 1 && s + 3 < 8) stage(s + 3);
    const char* base = smem + (s & 3) * K1_STAGE;
#pragma unroll
    for (int ks = 0; ks < 2; ++ks) {
      s16x8 af[4], bf[6];
#pragma unroll
      for (int mi = 0; mi < 4; ++mi) af[mi] = lds_b128(base + (aoff[mi] ^ (ks * 64)));
#pragma unroll
      for (int ni = 0; ni < 6; ++ni) {
        if constexpr (BT) bf[ni] = lds_b128(base + (boff[ni] ^ (ks * 64)));
        else {
          s16x4 lo = lds_tr(base + boff[ni] + ks * 32 * 384);
          s16x4 hi = lds_tr(base + boff[ni] + ks * 32 * 384 + 4 * 384);
          bf[ni] = cat(lo, hi);
        }
      }
#pragma unroll
      for (int ni = 0; ni < 6; ++ni)
#pragma unroll
        for (int mi = 0; mi < 4; ++mi) acc[ni][mi] = mfma16(bf[ni], af[mi], acc[ni][mi]);
#pragma unroll
      for (int mi = 0; mi < 2; ++mi) {
        u32x4 v = __builtin_bit_cast(u32x4, af[mi]);
#pragma unroll
        for (int j = 0; j < 4; ++j) {
          rs[mi] = dot2(v[j], 0x3f803f80u, rs[mi]);
          rq[mi] = dot2(v[j], v[j], rq[mi]);
        }
      }
#pragma unroll
      for (int ni = 0; ni < 3; ++ni) {
        u32x4 v = __builtin_bit_cast(u32x4, bf[ni]);
#pragma unroll
        for (int j = 0; j < 4; ++j) cs[ni] = dot2(v[j], 0x3f803f80u, cs[ni]);
      }
    }
  }
  BARRIER();
  float* st_mean = (float*)smem;
  float* st_rinv = (float*)smem + 128;
  float* st_cs = (float*)smem + 256;
#pragma unroll
  for (int mi = 0; mi < 2; ++mi) {
    float a = rs[mi], b = rq[mi];
    a += __shfl_xor(a, 16); a += __shfl_xor(a, 32);
    b += __shfl_xor(b, 16); b += __shfl_xor(b, 32);
    float mean = a * (1.0f / D_);
    float var = b * (1.0f / D_) - mean * mean;
    var = var < 0.f ? 0.f : var;
    float mb = rbf(mean);
    float sb = rbf(sqrtf(rbf(rbf(var) + 1e-5f)));
    int row = wm * 64 + (((mi + 2 * wn) & 3) * 16) + r16;
    if (g == 0) { st_mean[row] = mb; st_rinv[row] = 1.0f / sb; }
  }
#pragma unroll
  for (int ni = 0; ni < 3; ++ni) {
    float a = cs[ni];
    a += __shfl_xor(a, 16); a += __shfl_xor(a, 32);
    int nb = wn * 6 + ni + 3 * wm;
    if (g == 0) st_cs[nb * 16 + r16] = a;
  }
  BARRIER();
#pragma unroll
  for (int mi = 0; mi < 4; ++mi) {
    int rl = wm * 64 + (((mi + 2 * wn) & 3) * 16) + r16;
    float mean = st_mean[rl], rinv = st_rinv[rl];
    int row = m0 + rl;
#pragma unroll
    for (int ni = 0; ni < 6; ++ni) {
      int pb = ni + 3 * wm;
      pb = pb >= 6 ? pb - 6 : pb;
      int nl = (wn * 6 + pb) * 16 + 4 * g;
      f32x4 c4 = *(const f32x4*)(st_cs + nl);
      float v[4];
#pragma unroll
      for (int j = 0; j < 4; ++j) v[j] = (acc[ni][mi][j] - mean * c4[j]) * rinv;
      int n = n0 + nl;
      if constexpr (BT) {
        if (n < 1024) {
          u16* dst;
          if (n < 512) dst = AB + (size_t)row * D_ + n;
          else {
            dst = AB + (size_t)(S_ + row) * D_ + (n - 512);
#pragma unroll
            for (int j = 0; j < 4; ++j) v[j] = -v[j];
          }
          *(u32x2*)dst = (u32x2){pack2(v[0], v[1]), pack2(v[2], v[3])};
        }
      } else {
        if (n < F_) {
#pragma unroll
          for (int j = 0; j < 4; ++j) v[j] = v[j] > 0.f ? v[j] : 0.f;
          *(u32x2*)(H + (size_t)row * HLD + n) = (u32x2){pack2(v[0], v[1]), pack2(v[2], v[3])};
        }
      }
    }
  }
}

__global__ __launch_bounds__(256, 1) void k1_kernel(const u16* __restrict__ X, const u16* __restrict__ Hr,
                                                    const u16* __restrict__ Hi, const u16* __restrict__ W1,
                                                    u16* __restrict__ AB, u16* __restrict__ H,
                                                    unsigned* __restrict__ cnt) {
  extern __shared__ __attribute__((aligned(1024))) char smem[];
  const int bid = blockIdx.x;
  if (bid == 0 && threadIdx.x < 64) cnt[threadIdx.x * 16] = 0u;
  const int xcd = bid & 7, idx = bid >> 3;
  const int mt = (xcd & 1) * 8 + (idx & 7);
  const int nt = (xcd >> 1) * 4 + (idx >> 3);
  if (nt < 6) k1_body<true>(smem, X, Hr, Hi, W1, AB, H, mt * 128, nt * 192);
  else k1_body<false>(smem, X, Hr, Hi, W1, AB, H, mt * 128, (nt - 6) * 192);
}

constexpr int K2_STAGE = 32768;
constexpr int K2_LDS = 4 * K2_STAGE;
constexpr int K2_NS = 23;

__global__ __launch_bounds__(256, 1) void k2_kernel(const u16* __restrict__ Cr, const u16* __restrict__ Ci,
                                                    const u16* __restrict__ H, const u16* __restrict__ AB,
                                                    const u16* __restrict__ W2, const u16* __restrict__ X,
                                                    u16* __restrict__ out, float* __restrict__ xch,
                                                    unsigned* __restrict__ cnt) {
  extern __shared__ __attribute__((aligned(1024))) char smem[];
  const int tid = threadIdx.x, lane = tid & 63;
  const int w = __builtin_amdgcn_readfirstlane(tid >> 6);
  const int wm = w >> 1, wn = w & 1;
  const int bid = blockIdx.x;
  const int xcd = bid & 7, idx = bid >> 3;
  const int ks = xcd & 3, mh = xcd >> 2;
  const int mt = mh * 8 + (idx >> 2), nt = idx & 3;
  const int m0 = mt * 128, n0 = nt * 128;
  const int tile = mt * 4 + nt;

  unsigned aoffg[4], boffg[4];
#pragma unroll
  for (int i = 0; i < 4; ++i) {
    int row = (w * 4 + i) * 8 + (lane >> 3);
    int ch = (lane & 7) ^ (row & 7);
    aoffg[i] = (unsigned)((m0 + row) * 2048 * 2 + ch * 16);
    int k = (w * 4 + i) * 4 + (lane >> 4);
    int f = (k & 3) | (((k >> 3) & 1) << 2);
    int lc = (lane & 15) ^ (f << 1);
    boffg[i] = (unsigned)(k * 1024 + n0 * 2 + lc * 16);
  }
  auto stage = [&](int i) {
    int gs = ks * K2_NS + i;
    const char* abase;
    const char* bbase;
    if (gs < 32) abase = (const char*)Cr + gs * 128;
    else if (gs < 64) abase = (const char*)Ci + (gs - 32) * 128;
    else abase = (const char*)H + (gs - 64) * 128;
    if (gs < 64) bbase = (const char*)AB + (size_t)gs * 65536;
    else bbase = (const char*)W2 + (size_t)(gs - 64) * 65536;
    char* base = smem + (i & 3) * K2_STAGE;
#pragma unroll
    for (int j = 0; j < 4; ++j) glds16(abase + aoffg[j], base + (w * 4 + j) * 1024);
#pragma unroll
    for (int j = 0; j < 4; ++j) glds16(bbase + boffg[j], base + 16384 + (w * 4 + j) * 1024);
  };
  stage(0); stage(1); stage(2); stage(3);

  const int g = lane >> 4, r16 = lane & 15;
  int aoff[4];
#pragma unroll
  for (int mi = 0; mi < 4; ++mi) {
    int row = wm * 64 + (((mi + ks) & 3) * 16) + r16;
    aoff[mi] = row * 128 + ((g ^ (row & 7)) * 16);
  }
  int boff[4];
  {
    int q = r16 >> 2, p = lane & 3;
    int k = 8 * g + q;
    int f = q | ((g & 1) << 2);
#pragma unroll
    for (int ni = 0; ni < 4; ++ni) {
      int lc = wn * 8 + ni * 2 + (p >> 1);
      boff[ni] = 16384 + k * 256 + ((lc ^ (f << 1)) * 16) + 8 * (p & 1);
    }
  }
  f32x4 acc[4][4];
#pragma unroll
  for (int ni = 0; ni < 4; ++ni)
#pragma unroll
    for (int mi = 0; mi < 4; ++mi) acc[ni][mi] = (f32x4){0.f, 0.f, 0.f, 0.f};

  for (int i = 0; i < K2_NS; ++i) {
    if (i == 0) WAIT_V(24);
    else if (i + 2 < K2_NS) WAIT_V(16);
    else if (i + 1 < K2_NS) WAIT_V(8);
    else WAIT_V(0);
    BARRIER();
    if (i >= 1 && i + 3 < K2_NS) stage(i + 3);
    const char* base = smem + (i & 3) * K2_STAGE;
#pragma unroll
    for (int kk = 0; kk < 2; ++kk) {
      s16x8 af[4], bf[4];
#pragma unroll
      for (int mi = 0; mi < 4; ++mi) af[mi] = lds_b128(base + (aoff[mi] ^ (kk * 64)));
#pragma unroll
      for (int ni = 0; ni < 4; ++ni) {
        s16x4 lo = lds_tr(base + boff[ni] + kk * 8192);
        s16x4 hi = lds_tr(base + boff[ni] + kk * 8192 + 1024);
        bf[ni] = cat(lo, hi);
      }
#pragma unroll
      for (int ni = 0; ni < 4; ++ni)
#pragma unroll
        for (int mi = 0; mi < 4; ++mi) acc[ni][mi] = mfma16(bf[ni], af[mi], acc[ni][mi]);
    }
  }

  __amdgpu_buffer_rsrc_t xr = __builtin_amdgcn_make_buffer_rsrc((void*)xch, 0, 64 * 16 * 16384, 0x00020000);
#pragma unroll
  for (int mi = 1; mi < 4; ++mi) {
    int dest = (ks + mi) & 3;
    unsigned off = (unsigned)((((tile * 4 + dest) * 4 + ks) * 4 + w) * 4096 + lane * 16);
#pragma unroll
    for (int ni = 0; ni < 4; ++ni)
      __builtin_amdgcn_raw_buffer_store_b128(__builtin_bit_cast(u32x4, acc[ni][mi]), xr, off + ni * 1024, 0, 16);
  }
  const int orow = m0 + wm * 64 + ks * 16 + r16;
  u32x2 xres[4];
#pragma unroll
  for (int ni = 0; ni < 4; ++ni)
    xres[ni] = *(const u32x2*)(X + (size_t)orow * D_ + n0 + wn * 64 + ni * 16 + 4 * g);
  WAIT_V(0);
  __builtin_amdgcn_s_barrier();
  GAS unsigned* c = (GAS unsigned*)(cnt + tile * 16);
  if (tid == 0) __hip_atomic_fetch_add(c, 1u, __ATOMIC_RELAXED, __HIP_MEMORY_SCOPE_AGENT);
  for (unsigned spins = 0; spins < (1u << 22); ++spins) {
    unsigned v = __builtin_amdgcn_readfirstlane(__hip_atomic_load(c, __ATOMIC_RELAXED, __HIP_MEMORY_SCOPE_AGENT));
    if (v >= 4u) break;
    __builtin_amdgcn_s_sleep(1);
  }
  asm volatile("" ::: "memory");
  u32x4 pv[3][4];
#pragma unroll
  for (int j = 1; j < 4; ++j) {
    int src = (ks + j) & 3;
    unsigned off = (unsigned)((((tile * 4 + ks) * 4 + src) * 4 + w) * 4096 + lane * 16);
#pragma unroll
    for (int ni = 0; ni < 4; ++ni) pv[j - 1][ni] = __builtin_amdgcn_raw_buffer_load_b128(xr, off + ni * 1024, 0, 16);
  }
#pragma unroll
  for (int ni = 0; ni < 4; ++ni) {
    f32x4 v = acc[ni][0];
#pragma unroll
    for (int j = 0; j < 3; ++j) v += __builtin_bit_cast(f32x4, pv[j][ni]);
    v[0] += bf2f(xres[ni][0] & 0xffffu);
    v[1] += bf2f(xres[ni][0] >> 16);
    v[2] += bf2f(xres[ni][1] & 0xffffu);
    v[3] += bf2f(xres[ni][1] >> 16);
    *(u32x2*)(out + (size_t)orow * D_ + n0 + wn * 64 + ni * 16 + 4 * g) = (u32x2){pack2(v[0], v[1]), pack2(v[2], v[3])};
  }
}

extern "C" void kernel_launch(void* const* d_in, const int* in_sizes, int n_in, void* d_out, int out_size,
                              void* d_ws, size_t ws_size, hipStream_t stream) {
  const u16* X = (const u16*)d_in[0];
  const u16* Cr = (const u16*)d_in[1];
  const u16* Ci = (const u16*)d_in[2];
  const u16* Hr = (const u16*)d_in[3];
  const u16* Hi = (const u16*)d_in[4];
  const u16* W1 = (const u16*)d_in[6];
  const u16* W2 = (const u16*)d_in[7];
  char* ws = (char*)d_ws;
  unsigned* cnt = (unsigned*)ws;
  u16* AB = (u16*)(ws + 65536);
  u16* H = (u16*)(ws + 65536 + 4194304);
  float* xch = (float*)(ws + 65536 + 4194304 + 8388608);
  hipFuncSetAttribute((const void*)k1_kernel, hipFuncAttributeMaxDynamicSharedMemorySize, K1_LDS);
  hipFuncSetAttribute((const void*)k2_kernel, hipFuncAttributeMaxDynamicSharedMemorySize, K2_LDS);
  k1_kernel<<<256, 256, K1_LDS, stream>>>(X, Hr, Hi, W1, AB, H, cnt);
  k2_kernel<<<256, 256, K2_LDS, stream>>>(Cr, Ci, H, AB, W2, X, (u16*)d_out, xch, cnt);
}
```

```cpp
#include <hip/hip_runtime.h>
#include <stdint.h>

typedef short s16x8 __attribute__((ext_vector_type(8)));
typedef short s16x4 __attribute__((ext_vector_type(4)));
typedef __bf16 bf16x2 __attribute__((ext_vector_type(2)));
typedef float f32x4 __attribute__((ext_vector_type(4)));
typedef unsigned u32x4 __attribute__((ext_vector_type(4)));
typedef unsigned u32x2 __attribute__((ext_vector_type(2)));
typedef unsigned short u16;
#define LAS __attribute__((address_space(3)))
#define GAS __attribute__((address_space(1)))

constexpr int S_ = 2048, D_ = 512, F_ = 1792;
constexpr int HLD = 2048;

#define STAMP(buf, slot) do {} while (0)
#define WAIT_V(n) asm volatile("s_waitcnt vmcnt(%0)" ::"n"(n) : "memory")
#define WAIT_L0() asm volatile("s_waitcnt lgkmcnt(0)" ::: "memory")
#define BARRIER() do { WAIT_L0(); __builtin_amdgcn_s_barrier(); asm volatile("" ::: "memory"); } while (0)
#define RAWBAR() do { asm volatile("" ::: "memory"); __builtin_amdgcn_s_barrier(); asm volatile("" ::: "memory"); } while (0)

__device__ __forceinline__ float bf2f(unsigned h) { return __uint_as_float(h << 16); }
__device__ __forceinline__ float rbf(float f) {
  __bf16 b = (__bf16)f;
  return (float)b;
}
__device__ __forceinline__ unsigned pack2(float lo, float hi) {
  bf16x2 v;
  v[0] = (__bf16)lo;
  v[1] = (__bf16)hi;
  return __builtin_bit_cast(unsigned, v);
}
__device__ __forceinline__ void glds16(const void* g, unsigned lds) {
  asm volatile("s_mov_b32 m0, %1\n\ts_nop 0\n\tglobal_load_lds_dwordx4 %0, off" ::"v"(g), "s"(lds) : "memory");
}
__device__ __forceinline__ void glds16s(unsigned voff, const void* sbase, unsigned lds) {
  asm volatile("s_mov_b32 m0, %2\n\ts_nop 0\n\tglobal_load_lds_dwordx4 %0, %1" ::"v"(voff), "s"(sbase), "s"(lds) : "memory");
}
#define touch(g) asm volatile("global_load_dword %0, %1, off" : "+v"(sink) : "v"((const void*)(g)) : "memory")
#define TOUCH_END() do { WAIT_V(0); asm volatile("" ::"v"(sink)); } while (0)
__device__ __forceinline__ s16x8 lds_b128(const char* p) { return *(const LAS s16x8*)p; }
__device__ __forceinline__ s16x4 lds_tr(const char* p) {
  return __builtin_amdgcn_ds_read_tr16_b64_v4i16((LAS s16x4*)p);
}
__device__ __forceinline__ s16x8 cat(s16x4 a, s16x4 b) {
  return __builtin_shufflevector(a, b, 0, 1, 2, 3, 4, 5, 6, 7);
}
__device__ __forceinline__ f32x4 mfma16(s16x8 a, s16x8 b, f32x4 c) {
  return __builtin_amdgcn_mfma_f32_16x16x32_bf16(a, b, c, 0, 0, 0);
}
__device__ __forceinline__ float dot2(unsigned a, unsigned b, float c) {
  return __builtin_amdgcn_fdot2_f32_bf16(__builtin_bit_cast(bf16x2, a), __builtin_bit_cast(bf16x2, b), c, false);
}

constexpr unsigned XCH_BYTES = 64u * 4u * 3u * 4u * 2048u;

constexpr int K1_STAGE = 40960;
constexpr int K1_LDS = 4 * K1_STAGE;

template <bool BT>
__device__ __forceinline__ void k1_body(char* smem, const u16* __restrict__ X, const u16* __restrict__ Hr,
                                        const u16* __restrict__ Hi, const u16* __restrict__ W1,
                                        const u16* __restrict__ Cr, const u16* __restrict__ Ci,
                                        u16* __restrict__ AB, u16* __restrict__ H, char* __restrict__ xch,
                                        int m0, int n0, char* dbg) {
  const int tid = threadIdx.x, lane = tid & 63;
  const int wv = __builtin_amdgcn_readfirstlane(tid >> 6);
  const int w = wv & 3;
  const int wm = w >> 1, wn = w & 1;

  if (wv >= 4) {
    if (wv < 6) {
      const int pw = wv - 4;
      const char* aptr[8];
#pragma unroll
      for (int i = 0; i < 8; ++i) {
        int row = (pw * 8 + i) * 8 + (lane >> 3);
        int ch = (lane & 7) ^ (row & 7);
        aptr[i] = (const char*)(X + (size_t)(m0 + row) * D_) + ch * 16;
      }
      const char* bptr[12];
#pragma unroll
      for (int i = 0; i < 12; ++i) {
        int u = (pw * 12 + i) * 64 + lane;
        if constexpr (BT) {
          int row = u >> 3;
          int ch = (u & 7) ^ (row & 7);
          int n = n0 + row;
          n = n > 1023 ? 1023 : n;
          const u16* src = n < 512 ? Hr + (size_t)n * D_ : Hi + (size_t)(n - 512) * D_;
          bptr[i] = (const char*)src + ch * 16;
        } else {
          int k = u / 24, pc = u - k * 24;
          int f = ((k >> 1) & 1) | (((k >> 3) & 1) << 1);
          int lc = pc ^ (f << 1);
          int col = n0 + lc * 8;
          col = col > F_ - 8 ? F_ - 8 : col;
          bptr[i] = (const char*)(W1 + (size_t)k * F_ + col);
        }
      }
      constexpr int BSTEP = BT ? 128 : 64 * F_ * 2;
      const unsigned lbase = (unsigned)(size_t)(LAS char*)smem;
      auto stage = [&](int s) {
        unsigned base = lbase + (s & 3) * K1_STAGE;
#pragma unroll
        for (int i = 0; i < 8; ++i) glds16(aptr[i] + s * 128, base + (pw * 8 + i) * 1024);
#pragma unroll
        for (int i = 0; i < 12; ++i) glds16(bptr[i] + (size_t)s * BSTEP, base + 16384 + (pw * 12 + i) * 1024);
      };
      stage(0); stage(1); stage(2);
      WAIT_V(40);
      RAWBAR();
      stage(3);
      for (int s = 1; s < 8; ++s) {
        if (s <= 5) WAIT_V(40);
        else if (s == 6) WAIT_V(20);
        else WAIT_V(0);
        RAWBAR();
        if (s + 3 < 8) stage(s + 3);
      }
    } else if (wv == 6) {
      unsigned sink = 0;
      for (int s = 0; s < 6; ++s) RAWBAR();
#pragma unroll
      for (int i = 0; i < 8; ++i) {
        const u16* src = (i & 4) ? Ci : Cr;
        touch((const char*)src + (size_t)blockIdx.x * 32768 + ((i & 3) * 64 + lane) * 128);
      }
      for (int s = 6; s < 8; ++s) RAWBAR();
      TOUCH_END();
    } else {
      __amdgpu_buffer_rsrc_t xr = __builtin_amdgcn_make_buffer_rsrc((void*)xch, 0, XCH_BYTES, 0x00020000);
      u32x4 sent = {0xffffffffu, 0xffffffffu, 0xffffffffu, 0xffffffffu};
      for (int s = 0; s < 6; ++s) RAWBAR();
#pragma unroll
      for (int i = 0; i < 24; ++i)
        __builtin_amdgcn_raw_buffer_store_b128(sent, xr, blockIdx.x * 24576u + i * 1024u + lane * 16u, 0, 16);
      for (int s = 6; s < 8; ++s) RAWBAR();
    }
    return;
  }

  const int g = lane >> 4, r16 = lane & 15;
  int aoff[4];
#pragma unroll
  for (int mi = 0; mi < 4; ++mi) {
    int row = wm * 64 + (((mi + 2 * wn) & 3) * 16) + r16;
    aoff[mi] = row * 128 + ((g ^ (row & 7)) * 16);
  }
  int boff[6];
#pragma unroll
  for (int ni = 0; ni < 6; ++ni) {
    int pb = ni + 3 * wm;
    pb = pb >= 6 ? pb - 6 : pb;
    int nb = wn * 6 + pb;
    if constexpr (BT) {
      int row = nb * 16 + r16;
      boff[ni] = 16384 + row * 128 + ((g ^ (row & 7)) * 16);
    } else {
      int q = r16 >> 2, p = lane & 3;
      int k = 8 * g + q;
      int f = ((k >> 1) & 1) | (((k >> 3) & 1) << 1);
      int lc = nb * 2 + (p >> 1);
      boff[ni] = 16384 + k * 384 + ((lc ^ (f << 1)) * 16) + 8 * (p & 1);
    }
  }

  f32x4 acc[6][4];
#pragma unroll
  for (int ni = 0; ni < 6; ++ni)
#pragma unroll
    for (int mi = 0; mi < 4; ++mi) acc[ni][mi] = (f32x4){0.f, 0.f, 0.f, 0.f};
  f32x4 rs[2], rq[2], cs[3];
#pragma unroll
  for (int i = 0; i < 2; ++i) { rs[i] = (f32x4){0.f, 0.f, 0.f, 0.f}; rq[i] = (f32x4){0.f, 0.f, 0.f, 0.f}; }
#pragma unroll
  for (int i = 0; i < 3; ++i) cs[i] = (f32x4){0.f, 0.f, 0.f, 0.f};
  const s16x8 ones = {0x3f80, 0x3f80, 0x3f80, 0x3f80, 0x3f80, 0x3f80, 0x3f80, 0x3f80};
  s16x8 a0[4], b0[6], a1[4], b1[6];

#define K1_READ(AF, BF, BASE, KK)                                                        \
  do {                                                                                   \
    _Pragma("unroll") for (int ni = 0; ni < 6; ++ni) {                                   \
      if (ni < 4) AF[ni] = lds_b128((BASE) + (aoff[ni] ^ ((KK) * 64)));                  \
      if constexpr (BT) BF[ni] = lds_b128((BASE) + (boff[ni] ^ ((KK) * 64)));            \
      else {                                                                             \
        s16x4 lo = lds_tr((BASE) + boff[ni] + (KK) * 32 * 384);                          \
        s16x4 hi = lds_tr((BASE) + boff[ni] + (KK) * 32 * 384 + 4 * 384);                \
        BF[ni] = cat(lo, hi);                                                            \
      }                                                                                  \
    }                                                                                    \
  } while (0)
#define K1_MMA(AF, BF)                                                                   \
  do {                                                                                   \
    _Pragma("unroll") for (int ni = 0; ni < 6; ++ni)                                     \
      _Pragma("unroll") for (int mi = 0; mi < 4; ++mi)                                   \
        acc[ni][mi] = mfma16(BF[ni], AF[mi], acc[ni][mi]);                               \
    _Pragma("unroll") for (int mi = 0; mi < 2; ++mi) {                                   \
      rs[mi] = mfma16(ones, AF[mi], rs[mi]);                                             \
      rq[mi] = mfma16(AF[mi], AF[mi], rq[mi]);                                           \
    }                                                                                    \
    _Pragma("unroll") for (int ni = 0; ni < 3; ++ni) cs[ni] = mfma16(BF[ni], ones, cs[ni]); \
  } while (0)
#define K1_SCHED()                                                                       \
  do {                                                                                   \
    _Pragma("unroll") for (int q_ = 0; q_ < 16; ++q_) {                                  \
      __builtin_amdgcn_sched_group_barrier(0x008, 1, 0);                                 \
      __builtin_amdgcn_sched_group_barrier(0x100, 1, 0);                                 \
    }                                                                                    \
    __builtin_amdgcn_sched_group_barrier(0x008, 15, 0);                                  \
  } while (0)

  BARRIER();
  STAMP(dbg, 1);
  K1_READ(a0, b0, smem, 0);
  for (int s = 0; s < 7; ++s) {
    const char* base = smem + (s & 3) * K1_STAGE;
    const char* nbase = smem + ((s + 1) & 3) * K1_STAGE;
    K1_READ(a1, b1, base, 1);
    K1_MMA(a0, b0);
    K1_SCHED();
    __builtin_amdgcn_sched_barrier(0);
    BARRIER();
    K1_READ(a0, b0, nbase, 0);
    K1_MMA(a1, b1);
    K1_SCHED();
    __builtin_amdgcn_sched_barrier(0);
  }
  {
    const char* base = smem + 3 * K1_STAGE;
    K1_READ(a1, b1, base, 1);
    K1_MMA(a0, b0);
    K1_MMA(a1, b1);
  }
  STAMP(dbg, 2);
  float* st_mean = (float*)smem;
  float* st_rinv = (float*)smem + 128;
  float* st_cs = (float*)smem + 256;
#pragma unroll
  for (int mi = 0; mi < 2; ++mi) {
    float b = (r16 & 2) ? ((r16 & 1) ? rq[mi][3] : rq[mi][2]) : ((r16 & 1) ? rq[mi][1] : rq[mi][0]);
    float mean = rs[mi][0] * (1.0f / D_);
    float var = b * (1.0f / D_) - mean * mean;
    var = var < 0.f ? 0.f : var;
    float mb = rbf(mean);
    float sb = rbf(sqrtf(rbf(rbf(var) + 1e-5f)));
    int row = wm * 64 + (((mi + 2 * wn) & 3) * 16) + r16;
    if (g == (r16 >> 2)) { st_mean[row] = mb; st_rinv[row] = 1.0f / sb; }
  }
#pragma unroll
  for (int ni = 0; ni < 3; ++ni) {
    int nb = wn * 6 + ni + 3 * wm;
    if (r16 == 0) *(f32x4*)(st_cs + nb * 16 + 4 * g) = cs[ni];
  }
  BARRIER();
  char* ot = smem + K1_STAGE;
#pragma unroll
  for (int mi = 0; mi < 4; ++mi) {
    int rl = wm * 64 + (((mi + 2 * wn) & 3) * 16) + r16;
    float mean = st_mean[rl], rinv = st_rinv[rl];
#pragma unroll
    for (int ni = 0; ni < 6; ++ni) {
      int pb = ni + 3 * wm;
      pb = pb >= 6 ? pb - 6 : pb;
      int nl = (wn * 6 + pb) * 16 + 4 * g;
      f32x4 c4 = *(const f32x4*)(st_cs + nl);
      float v[4];
#pragma unroll
      for (int j = 0; j < 4; ++j) v[j] = (acc[ni][mi][j] - mean * c4[j]) * rinv;
      if constexpr (BT) {
        if (n0 + nl >= 512) {
#pragma unroll
          for (int j = 0; j < 4; ++j) v[j] = -v[j];
        }
      } else {
#pragma unroll
        for (int j = 0; j < 4; ++j) v[j] = v[j] > 0.f ? v[j] : 0.f;
      }
      *(u32x2*)(ot + rl * 400 + nl * 2) = (u32x2){pack2(v[0], v[1]), pack2(v[2], v[3])};
    }
  }
  BARRIER();
#pragma unroll
  for (int i = 0; i < 12; ++i) {
    int u = i * 256 + tid;
    int rl = u / 24, c = u - rl * 24;
    u32x4 v = *(const u32x4*)(ot + rl * 400 + c * 16);
    int n = n0 + c * 8, row = m0 + rl;
    if constexpr (BT) {
      if (n < 1024) {
        u16* dst = n < 512 ? AB + (size_t)row * D_ + n : AB + (size_t)(S_ + row) * D_ + (n - 512);
        *(u32x4*)dst = v;
      }
    } else {
      if (n < F_) *(u32x4*)(H + (size_t)row * HLD + n) = v;
    }
  }
}

__global__ __launch_bounds__(512, 1) void k1_kernel(const u16* __restrict__ X, const u16* __restrict__ Hr,
                                                    const u16* __restrict__ Hi, const u16* __restrict__ W1,
                                                    const u16* __restrict__ Cr, const u16* __restrict__ Ci,
                                                    u16* __restrict__ AB, u16* __restrict__ H,
                                                    char* __restrict__ xch) {
  extern __shared__ __attribute__((aligned(1024))) char smem[];
  const int bid = blockIdx.x;
  char* dbg = xch + (17u << 20);
  STAMP(dbg, 0);
  const int xcd = bid & 7, idx = bid >> 3;
  const int mt = (xcd & 1) * 8 + (idx & 7);
  const int nt = (xcd >> 1) * 4 + (idx >> 3);
  if (nt < 6) k1_body<true>(smem, X, Hr, Hi, W1, Cr, Ci, AB, H, xch, mt * 128, nt * 192, dbg);
  else k1_body<false>(smem, X, Hr, Hi, W1, Cr, Ci, AB, H, xch, mt * 128, (nt - 6) * 192, dbg);
}

constexpr int K2_STAGE = 32768;
constexpr int K2_LDS = 4 * K2_STAGE;
constexpr int K2_NS = 23;
constexpr int K2_PD = 10;

__global__ __launch_bounds__(512, 1) void k2_kernel(const u16* __restrict__ Cr, const u16* __restrict__ Ci,
                                                    const u16* __restrict__ H, const u16* __restrict__ AB,
                                                    const u16* __restrict__ W2, const u16* __restrict__ X,
                                                    u16* __restrict__ out, char* __restrict__ xch) {
  extern __shared__ __attribute__((aligned(1024))) char smem[];
  const int tid = threadIdx.x, lane = tid & 63;
  const int wv = __builtin_amdgcn_readfirstlane(tid >> 6);
  const int w = wv & 3;
  const int wm = w >> 1, wn = w & 1;
  const int bid = blockIdx.x;
  const int xcd = bid & 7, idx = bid >> 3;
  const int ks = xcd & 3, mh = xcd >> 2;
  const int mt = mh * 8 + (idx >> 2), nt = idx & 3;
  const int m0 = mt * 128, n0 = nt * 128;
  const int tile = mt * 4 + nt;
  char* dbg = xch + (16u << 20);
  STAMP(dbg, 0);

  auto abase_of = [&](int i) -> const char* {
    int gs = ks * K2_NS + i;
    if (gs < 32) return (const char*)Cr + gs * 128;
    if (gs < 64) return (const char*)Ci + (gs - 32) * 128;
    return (const char*)H + (gs - 64) * 128;
  };
  auto bbase_of = [&](int i) -> const char* {
    int gs = ks * K2_NS + i;
    if (gs < 64) return (const char*)AB + (size_t)gs * 65536;
    return (const char*)W2 + (size_t)(gs - 64) * 65536;
  };

  if (wv >= 4) {
    if (wv < 6) {
      const int pw = wv - 4;
      unsigned aoffg[8], boffg[8];
#pragma unroll
      for (int i = 0; i < 8; ++i) {
        int row = (pw * 8 + i) * 8 + (lane >> 3);
        int ch = (lane & 7) ^ (row & 7);
        aoffg[i] = (unsigned)((m0 + row) * 2048 * 2 + ch * 16);
        int k = (pw * 8 + i) * 4 + (lane >> 4);
        int f = (k & 3) | (((k >> 3) & 1) << 2);
        int lc = (lane & 15) ^ (f << 1);
        boffg[i] = (unsigned)(k * 1024 + n0 * 2 + lc * 16);
      }
      const unsigned lbase = (unsigned)(size_t)(LAS char*)smem + pw * 8192;
      auto stage = [&](int i) {
        const char* abase = abase_of(i);
        const char* bbase = bbase_of(i);
        unsigned base = lbase + (i & 3) * K2_STAGE;
#pragma unroll
        for (int j = 0; j < 8; ++j) glds16s(aoffg[j], abase, base + j * 1024);
#pragma unroll
        for (int j = 0; j < 8; ++j) glds16s(boffg[j], bbase, base + 16384 + j * 1024);
      };
      stage(0); stage(1); stage(2);
      WAIT_V(32);
      RAWBAR();
      stage(3);
      for (int i = 1; i < K2_NS; ++i) {
        if (i + 2 < K2_NS) WAIT_V(32);
        else if (i + 1 < K2_NS) WAIT_V(16);
        else WAIT_V(0);
        RAWBAR();
        if (i + 3 < K2_NS) stage(i + 3);
      }
    }
    return;
  }

  const int g = lane >> 4, r16 = lane & 15;
  int aoff[4];
#pragma unroll
  for (int mi = 0; mi < 4; ++mi) {
    int row = wm * 64 + (((mi + ks) & 3) * 16) + r16;
    aoff[mi] = row * 128 + ((g ^ (row & 7)) * 16);
  }
  int boff[4];
  {
    int q = r16 >> 2, p = lane & 3;
    int k = 8 * g + q;
    int f = q | ((g & 1) << 2);
#pragma unroll
    for (int ni = 0; ni < 4; ++ni) {
      int lc = wn * 8 + ni * 2 + (p >> 1);
      boff[ni] = 16384 + k * 256 + ((lc ^ (f << 1)) * 16) + 8 * (p & 1);
    }
  }
  f32x4 acc[4][4];
#pragma unroll
  for (int ni = 0; ni < 4; ++ni)
#pragma unroll
    for (int mi = 0; mi < 4; ++mi) acc[ni][mi] = (f32x4){0.f, 0.f, 0.f, 0.f};

  const int orow = m0 + wm * 64 + ks * 16 + r16;
  u32x2 xres[4];
#pragma unroll
  for (int ni = 0; ni < 4; ++ni)
    xres[ni] = *(const u32x2*)(X + (size_t)orow * D_ + n0 + wn * 64 + ni * 16 + 4 * g);

  s16x8 a0[4], b0[4], a1[4], b1[4];
#define K2_READ(AF, BF, BASE, KK)                                                  \
  do {                                                                             \
    _Pragma("unroll") for (int mi = 0; mi < 4; ++mi) {                             \
      AF[mi] = lds_b128((BASE) + (aoff[mi] ^ ((KK) * 64)));                        \
      s16x4 lo = lds_tr((BASE) + boff[mi] + (KK) * 8192);                          \
      s16x4 hi = lds_tr((BASE) + boff[mi] + (KK) * 8192 + 1024);                   \
      BF[mi] = cat(lo, hi);                                                        \
    }                                                                              \
  } while (0)
#define K2_MMA(AF, BF)                                                             \
  do {                                                                             \
    _Pragma("unroll") for (int ni = 0; ni < 4; ++ni)                               \
      _Pragma("unroll") for (int mi = 0; mi < 4; ++mi)                             \
        acc[ni][mi] = mfma16(BF[ni], AF[mi], acc[ni][mi]);                         \
  } while (0)
#define K2_SCHED()                                                                 \
  do {                                                                             \
    _Pragma("unroll") for (int q_ = 0; q_ < 12; ++q_) {                            \
      __builtin_amdgcn_sched_group_barrier(0x008, 1, 0);                           \
      __builtin_amdgcn_sched_group_barrier(0x100, 1, 0);                           \
    }                                                                              \
    __builtin_amdgcn_sched_group_barrier(0x008, 4, 0);                             \
  } while (0)

  BARRIER();
  STAMP(dbg, 1);
  K2_READ(a0, b0, smem, 0);
  for (int i = 0; i < K2_NS - 1; ++i) {
    const char* base = smem + (i & 3) * K2_STAGE;
    const char* nbase = smem + ((i + 1) & 3) * K2_STAGE;
    K2_READ(a1, b1, base, 1);
    K2_MMA(a0, b0);
    K2_SCHED();
    __builtin_amdgcn_sched_barrier(0);
    BARRIER();
    K2_READ(a0, b0, nbase, 0);
    K2_MMA(a1, b1);
    K2_SCHED();
    __builtin_amdgcn_sched_barrier(0);
  }
  {
    const char* base = smem + ((K2_NS - 1) & 3) * K2_STAGE;
    K2_READ(a1, b1, base, 1);
    K2_MMA(a0, b0);
    K2_MMA(a1, b1);
  }
  STAMP(dbg, 2);

  __amdgpu_buffer_rsrc_t xr = __builtin_amdgcn_make_buffer_rsrc((void*)xch, 0, XCH_BYTES, 0x00020000);
#pragma unroll
  for (int mi = 1; mi < 4; ++mi) {
    int dest = (ks + mi) & 3;
    unsigned off = (unsigned)((((tile * 4 + dest) * 3 + (3 - mi)) * 4 + w) * 2048 + lane * 16);
#pragma unroll
    for (int hf = 0; hf < 2; ++hf) {
      u32x4 v;
#pragma unroll
      for (int q = 0; q < 2; ++q) {
        f32x4 a = acc[hf * 2 + q][mi];
        unsigned p0 = pack2(a[0], a[1]), p1 = pack2(a[2], a[3]);
        v[q * 2] = p0 < 0xfffffffeu ? p0 : 0xfffffffeu;
        v[q * 2 + 1] = p1 < 0xfffffffeu ? p1 : 0xfffffffeu;
      }
      __builtin_amdgcn_raw_buffer_store_b128(v, xr, off + hf * 1024, 0, 16);
    }
  }
  STAMP(dbg, 3);
  u32x4 pv[3][2];
  {
    unsigned off = (unsigned)(((tile * 4 + ks) * 3 * 4 + w) * 2048 + lane * 16);
    for (unsigned spins = 0; spins < (1u << 20); ++spins) {
      bool ok = true;
      asm volatile("" ::: "memory");
#pragma unroll
      for (int j = 0; j < 3; ++j)
#pragma unroll
        for (int hf = 0; hf < 2; ++hf) {
          pv[j][hf] = __builtin_amdgcn_raw_buffer_load_b128(xr, off + j * 8192 + hf * 1024, 0, 16);
        }
#pragma unroll
      for (int j = 0; j < 3; ++j)
#pragma unroll
        for (int hf = 0; hf < 2; ++hf)
#pragma unroll
          for (int e = 0; e < 4; ++e) ok &= pv[j][hf][e] != 0xffffffffu;
      if (__all(ok)) break;
      __builtin_amdgcn_s_sleep(2);
    }
  }
  STAMP(dbg, 4);
#pragma unroll
  for (int ni = 0; ni < 4; ++ni) {
    f32x4 v = acc[ni][0];
#pragma unroll
    for (int j = 0; j < 3; ++j) {
      unsigned p0 = pv[j][ni >> 1][(ni & 1) * 2], p1 = pv[j][ni >> 1][(ni & 1) * 2 + 1];
      v[0] += bf2f(p0 & 0xffffu);
      v[1] += bf2f(p0 >> 16);
      v[2] += bf2f(p1 & 0xffffu);
      v[3] += bf2f(p1 >> 16);
    }
    v[0] += bf2f(xres[ni][0] & 0xffffu);
    v[1] += bf2f(xres[ni][0] >> 16);
    v[2] += bf2f(xres[ni][1] & 0xffffu);
    v[3] += bf2f(xres[ni][1] >> 16);
    *(u32x2*)(out + (size_t)orow * D_ + n0 + wn * 64 + ni * 16 + 4 * g) = (u32x2){pack2(v[0], v[1]), pack2(v[2], v[3])};
  }
}

extern "C" void kernel_launch(void* const* d_in, const int* in_sizes, int n_in, void* d_out, int out_size,
                              void* d_ws, size_t ws_size, hipStream_t stream) {
  const u16* X = (const u16*)d_in[0];
  const u16* Cr = (const u16*)d_in[1];
  const u16* Ci = (const u16*)d_in[2];
  const u16* Hr = (const u16*)d_in[3];
  const u16* Hi = (const u16*)d_in[4];
  const u16* W1 = (const u16*)d_in[6];
  const u16* W2 = (const u16*)d_in[7];
  char* ws = (char*)d_ws;
  u16* AB = (u16*)(ws);
  u16* H = (u16*)(ws + 4194304);
  char* xch = ws + 4194304 + 8388608;
  (void)hipFuncSetAttribute((const void*)k1_kernel, hipFuncAttributeMaxDynamicSharedMemorySize, K1_LDS);
  (void)hipFuncSetAttribute((const void*)k2_kernel, hipFuncAttributeMaxDynamicSharedMemorySize, K2_LDS);
  k1_kernel<<<256, 512, K1_LDS, stream>>>(X, Hr, Hi, W1, Cr, Ci, AB, H, xch);
  k2_kernel<<<256, 512, K2_LDS, stream>>>(Cr, Ci, H, AB, W2, X, (u16*)d_out, xch);
}
```

```cpp
#include <hip/hip_runtime.h>
#include <stdint.h>

typedef short s16x8 __attribute__((ext_vector_type(8)));
typedef short s16x4 __attribute__((ext_vector_type(4)));
typedef __bf16 bf16x2 __attribute__((ext_vector_type(2)));
typedef float f32x4 __attribute__((ext_vector_type(4)));
typedef unsigned u32x4 __attribute__((ext_vector_type(4)));
typedef unsigned u32x2 __attribute__((ext_vector_type(2)));
typedef unsigned short u16;
#define LAS __attribute__((address_space(3)))
#define GAS __attribute__((address_space(1)))

constexpr int S_ = 2048, D_ = 512, F_ = 1792;
constexpr int HLD = 2048;

#define STAMP(buf, slot) do {} while (0)
#define WAIT_V(n) asm volatile("s_waitcnt vmcnt(%0)" ::"n"(n) : "memory")
#define WAIT_L0() asm volatile("s_waitcnt lgkmcnt(0)" ::: "memory")
#define BARRIER() do { WAIT_L0(); __builtin_amdgcn_s_barrier(); asm volatile("" ::: "memory"); } while (0)
#define RAWBAR() do { asm volatile("" ::: "memory"); __builtin_amdgcn_s_barrier(); asm volatile("" ::: "memory"); } while (0)

__device__ __forceinline__ float bf2f(unsigned h) { return __uint_as_float(h << 16); }
__device__ __forceinline__ float rbf(float f) {
  __bf16 b = (__bf16)f;
  return (float)b;
}
__device__ __forceinline__ unsigned pack2(float lo, float hi) {
  bf16x2 v;
  v[0] = (__bf16)lo;
  v[1] = (__bf16)hi;
  return __builtin_bit_cast(unsigned, v);
}
__device__ __forceinline__ void glds16(const void* g, unsigned lds) {
  asm volatile("s_mov_b32 m0, %1\n\ts_nop 0\n\tglobal_load_lds_dwordx4 %0, off" ::"v"(g), "s"(lds) : "memory");
}
__device__ __forceinline__ const char* uni(const char* p) {
  unsigned long long v = (unsigned long long)p;
  unsigned lo = __builtin_amdgcn_readfirstlane((unsigned)v), hi = __builtin_amdgcn_readfirstlane((unsigned)(v >> 32));
  return (const char*)(((unsigned long long)hi << 32) | lo);
}
__device__ __forceinline__ void glds16s(unsigned voff, const void* sbase, unsigned lds) {
  asm volatile("s_mov_b32 m0, %2\n\ts_nop 0\n\tglobal_load_lds_dwordx4 %0, %1" ::"v"(voff), "s"(sbase), "s"(lds) : "memory");
}
#define touch(g) asm volatile("global_load_dword %0, %1, off" : "+v"(sink) : "v"((const void*)(g)) : "memory")
#define TOUCH_END() do { WAIT_V(0); asm volatile("" ::"v"(sink)); } while (0)
__device__ __forceinline__ s16x8 lds_b128(const char* p) { return *(const LAS s16x8*)p; }
__device__ __forceinline__ s16x4 lds_tr(const char* p) {
  return __builtin_amdgcn_ds_read_tr16_b64_v4i16((LAS s16x4*)p);
}
__device__ __forceinline__ s16x8 cat(s16x4 a, s16x4 b) {
  return __builtin_shufflevector(a, b, 0, 1, 2, 3, 4, 5, 6, 7);
}
__device__ __forceinline__ f32x4 mfma16(s16x8 a, s16x8 b, f32x4 c) {
  return __builtin_amdgcn_mfma_f32_16x16x32_bf16(a, b, c, 0, 0, 0);
}
__device__ __forceinline__ float dot2(unsigned a, unsigned b, float c) {
  return __builtin_amdgcn_fdot2_f32_bf16(__builtin_bit_cast(bf16x2, a), __builtin_bit_cast(bf16x2, b), c, false);
}

constexpr unsigned XCH_BYTES = 64u * 4u * 3u * 4u * 2048u;
constexpr unsigned FLAG_BYTES = 32768u;

constexpr int K1_STAGE = 40960;
constexpr int K1_LDS = 4 * K1_STAGE;

template <bool BT>
__device__ __forceinline__ void k1_body(char* smem, const u16* __restrict__ X, const u16* __restrict__ Hr,
                                        const u16* __restrict__ Hi, const u16* __restrict__ W1,
                                        const u16* __restrict__ Cr, const u16* __restrict__ Ci, const u16* __restrict__ W2,
                                        u16* __restrict__ AB, u16* __restrict__ H, char* __restrict__ xch,
                                        int m0, int n0, char* dbg) {
  const int tid = threadIdx.x, lane = tid & 63;
  const int wv = __builtin_amdgcn_readfirstlane(tid >> 6);
  const int w = wv & 3;
  const int wm = w >> 1, wn = w & 1;

  if (wv >= 4) {
    const int pw = wv - 4;
    const char* aptr[4];
#pragma unroll
    for (int i = 0; i < 4; ++i) {
      int row = (pw * 4 + i) * 8 + (lane >> 3);
      int ch = (lane & 7) ^ (row & 7);
      aptr[i] = (const char*)(X + (size_t)(m0 + row) * D_) + ch * 16;
    }
    const char* bptr[6];
#pragma unroll
    for (int i = 0; i < 6; ++i) {
      int u = (pw * 6 + i) * 64 + lane;
      if constexpr (BT) {
        int row = u >> 3;
        int ch = (u & 7) ^ (row & 7);
        int n = n0 + row;
        n = n > 1023 ? 1023 : n;
        const u16* src = n < 512 ? Hr + (size_t)n * D_ : Hi + (size_t)(n - 512) * D_;
        bptr[i] = (const char*)src + ch * 16;
      } else {
        int k = u / 24, pc = u - k * 24;
        int f = ((k >> 1) & 1) | (((k >> 3) & 1) << 1);
        int lc = pc ^ (f << 1);
        int col = n0 + lc * 8;
        col = col > F_ - 8 ? F_ - 8 : col;
        bptr[i] = (const char*)(W1 + (size_t)k * F_ + col);
      }
    }
    constexpr int BSTEP = BT ? 128 : 64 * F_ * 2;
    const unsigned lbase = (unsigned)(size_t)(LAS char*)smem;
    const int rj = ((blockIdx.x >> 3) + (blockIdx.x >> 6)) & 3;
    auto stage = [&](int s) {
      unsigned base = lbase + (s & 3) * K1_STAGE;
      int ks_ = (s & ~3) | ((s + rj) & 3);
#pragma unroll
      for (int i = 0; i < 4; ++i) glds16(aptr[i] + ks_ * 128, base + (pw * 4 + i) * 1024);
#pragma unroll
      for (int i = 0; i < 6; ++i) glds16(bptr[i] + (size_t)ks_ * BSTEP, base + 16384 + (pw * 6 + i) * 1024);
    };
    unsigned sink = 0;
    stage(0); stage(1); stage(2);
    WAIT_V(20);
    RAWBAR();
    stage(3);
    for (int s = 1; s < 5; ++s) {
      WAIT_V(20);
      RAWBAR();
      stage(s + 3);
    }
    auto extras = [&]() {
      const int kq = blockIdx.x & 3, mh = (blockIdx.x >> 2) & 1, idx = blockIdx.x >> 3;
#pragma unroll
      for (int i = 0; i < 2; ++i) {
        const u16* src = (pw & 1) ? Ci : Cr;
        int ln = ((pw >> 1) * 2 + i) * 64 + lane;
        touch((const char*)src + (size_t)(mh * 1024 + idx * 32 + (ln >> 3)) * 4096 + kq * 1024 + (ln & 7) * 128);
      }
      {
        int ln = pw * 28 + (lane < 28 ? lane : 27);
        touch((const char*)W2 + (size_t)kq * 458752 + (idx * 112 + ln) * 128);
      }
      __amdgpu_buffer_rsrc_t xr = __builtin_amdgcn_make_buffer_rsrc((void*)xch, 0, XCH_BYTES + FLAG_BYTES, 0x00020000);
      if (lane < 8) __builtin_amdgcn_raw_buffer_store_b32(0u, xr, XCH_BYTES + blockIdx.x * 128u + pw * 32u + lane * 4u, 0, 16);
    };
    extras();
    WAIT_V(24);
    RAWBAR();
    WAIT_V(14);
    RAWBAR();
    WAIT_V(4);
    RAWBAR();
    RAWBAR();
    TOUCH_END();
    return;
  }

  const int g = lane >> 4, r16 = lane & 15;
  int aoff[4];
#pragma unroll
  for (int mi = 0; mi < 4; ++mi) {
    int row = wm * 64 + (((mi + 2 * wn) & 3) * 16) + r16;
    aoff[mi] = row * 128 + ((g ^ (row & 7)) * 16);
  }
  int boff[6];
#pragma unroll
  for (int ni = 0; ni < 6; ++ni) {
    int pb = ni + 3 * wm;
    pb = pb >= 6 ? pb - 6 : pb;
    int nb = wn * 6 + pb;
    if constexpr (BT) {
      int row = nb * 16 + r16;
      boff[ni] = 16384 + row * 128 + ((g ^ (row & 7)) * 16);
    } else {
      int q = r16 >> 2, p = lane & 3;
      int k = 8 * g + q;
      int f = ((k >> 1) & 1) | (((k >> 3) & 1) << 1);
      int lc = nb * 2 + (p >> 1);
      boff[ni] = 16384 + k * 384 + ((lc ^ (f << 1)) * 16) + 8 * (p & 1);
    }
  }

  f32x4 acc[6][4];
#pragma unroll
  for (int ni = 0; ni < 6; ++ni)
#pragma unroll
    for (int mi = 0; mi < 4; ++mi) acc[ni][mi] = (f32x4){0.f, 0.f, 0.f, 0.f};
  f32x4 rs[2], rq[2], cs[3];
#pragma unroll
  for (int i = 0; i < 2; ++i) { rs[i] = (f32x4){0.f, 0.f, 0.f, 0.f}; rq[i] = (f32x4){0.f, 0.f, 0.f, 0.f}; }
#pragma unroll
  for (int i = 0; i < 3; ++i) cs[i] = (f32x4){0.f, 0.f, 0.f, 0.f};
  const s16x8 ones = {0x3f80, 0x3f80, 0x3f80, 0x3f80, 0x3f80, 0x3f80, 0x3f80, 0x3f80};
  s16x8 a0[4], b0[6], a1[4], b1[6];

#define K1_READ(AF, BF, BASE, KK)                                                        \
  do {                                                                                   \
    _Pragma("unroll") for (int ni = 0; ni < 6; ++ni) {                                   \
      if (ni < 4) AF[ni] = lds_b128((BASE) + (aoff[ni] ^ ((KK) * 64)));                  \
      if constexpr (BT) BF[ni] = lds_b128((BASE) + (boff[ni] ^ ((KK) * 64)));            \
      else {                                                                             \
        s16x4 lo = lds_tr((BASE) + boff[ni] + (KK) * 32 * 384);                          \
        s16x4 hi = lds_tr((BASE) + boff[ni] + (KK) * 32 * 384 + 4 * 384);                \
        BF[ni] = cat(lo, hi);                                                            \
      }                                                                                  \
    }                                                                                    \
  } while (0)
#define K1_MMA(AF, BF)                                                                   \
  do {                                                                                   \
    _Pragma("unroll") for (int ni = 0; ni < 6; ++ni)                                     \
      _Pragma("unroll") for (int mi = 0; mi < 4; ++mi)                                   \
        acc[ni][mi] = mfma16(BF[ni], AF[mi], acc[ni][mi]);                               \
    _Pragma("unroll") for (int mi = 0; mi < 2; ++mi) {                                   \
      rs[mi] = mfma16(ones, AF[mi], rs[mi]);                                             \
      rq[mi] = mfma16(AF[mi], AF[mi], rq[mi]);                                           \
    }                                                                                    \
    _Pragma("unroll") for (int ni = 0; ni < 3; ++ni) cs[ni] = mfma16(BF[ni], ones, cs[ni]); \
  } while (0)
#define K1_SCHED()                                                                       \
  do {                                                                                   \
    _Pragma("unroll") for (int q_ = 0; q_ < 16; ++q_) {                                  \
      __builtin_amdgcn_sched_group_barrier(0x008, 1, 0);                                 \
      __builtin_amdgcn_sched_group_barrier(0x100, 1, 0);                                 \
    }                                                                                    \
    __builtin_amdgcn_sched_group_barrier(0x008, 15, 0);                                  \
  } while (0)

  BARRIER();
  STAMP(dbg, 1);
  K1_READ(a0, b0, smem, 0);
  for (int s = 0; s < 7; ++s) {
    const char* base = smem + (s & 3) * K1_STAGE;
    const char* nbase = smem + ((s + 1) & 3) * K1_STAGE;
    K1_READ(a1, b1, base, 1);
    K1_MMA(a0, b0);
    K1_SCHED();
    __builtin_amdgcn_sched_barrier(0);
    BARRIER();
    K1_READ(a0, b0, nbase, 0);
    K1_MMA(a1, b1);
    K1_SCHED();
    __builtin_amdgcn_sched_barrier(0);
  }
  {
    const char* base = smem + 3 * K1_STAGE;
    K1_READ(a1, b1, base, 1);
    K1_MMA(a0, b0);
    K1_MMA(a1, b1);
  }
  STAMP(dbg, 2);
  float* st_mean = (float*)smem;
  float* st_rinv = (float*)smem + 128;
  float* st_cs = (float*)smem + 256;
#pragma unroll
  for (int mi = 0; mi < 2; ++mi) {
    float b = (r16 & 2) ? ((r16 & 1) ? rq[mi][3] : rq[mi][2]) : ((r16 & 1) ? rq[mi][1] : rq[mi][0]);
    float mean = rs[mi][0] * (1.0f / D_);
    float var = b * (1.0f / D_) - mean * mean;
    var = var < 0.f ? 0.f : var;
    float mb = rbf(mean);
    float sb = rbf(sqrtf(rbf(rbf(var) + 1e-5f)));
    int row = wm * 64 + (((mi + 2 * wn) & 3) * 16) + r16;
    if (g == (r16 >> 2)) { st_mean[row] = mb; st_rinv[row] = 1.0f / sb; }
  }
#pragma unroll
  for (int ni = 0; ni < 3; ++ni) {
    int nb = wn * 6 + ni + 3 * wm;
    if (r16 == 0) *(f32x4*)(st_cs + nb * 16 + 4 * g) = cs[ni];
  }
  BARRIER();
  STAMP(dbg, 3);
  {
    typedef float f32x2 __attribute__((ext_vector_type(2)));
    typedef short s16x2 __attribute__((ext_vector_type(2)));
    float nmean[4], rinv[4];
#pragma unroll
    for (int mi = 0; mi < 4; ++mi) {
      int rl = wm * 64 + (((mi + 2 * wn) & 3) * 16) + r16;
      nmean[mi] = -st_mean[rl];
      rinv[mi] = st_rinv[rl];
    }
#pragma unroll
    for (int np = 0; np < 3; ++np) {
      int pb0 = 2 * np + 3 * wm, pb1 = 2 * np + 1 + 3 * wm;
      pb0 = pb0 >= 6 ? pb0 - 6 : pb0;
      pb1 = pb1 >= 6 ? pb1 - 6 : pb1;
      const int nb0 = (wn * 6 + pb0) * 16, nb1 = (wn * 6 + pb1) * 16;
      f32x4 c0 = *(const f32x4*)(st_cs + nb0 + 4 * g), c1 = *(const f32x4*)(st_cs + nb1 + 4 * g);
      const int n = n0 + ((g & 1) ? nb1 : nb0) + (g >> 1) * 8;
      float sg0 = 1.f, sg1 = 1.f;
      if constexpr (BT) { sg0 = n0 + nb0 >= 512 ? -1.f : 1.f; sg1 = n0 + nb1 >= 512 ? -1.f : 1.f; }
#pragma unroll
      for (int mi = 0; mi < 4; ++mi) {
        unsigned pk[2][2];
#pragma unroll
        for (int h = 0; h < 2; ++h) {
          f32x4 a = acc[2 * np + h][mi];
          f32x4 cc = h ? c1 : c0;
          float r = BT ? rinv[mi] * (h ? sg1 : sg0) : rinv[mi];
          f32x2 m2 = {nmean[mi], nmean[mi]}, r2 = {r, r};
          f32x2 lo = ((f32x2){cc[0], cc[1]} * m2 + (f32x2){a[0], a[1]}) * r2;
          f32x2 hi = ((f32x2){cc[2], cc[3]} * m2 + (f32x2){a[2], a[3]}) * r2;
          pk[h][0] = pack2(lo[0], lo[1]);
          pk[h][1] = pack2(hi[0], hi[1]);
          if constexpr (!BT) {
            const s16x2 z = {0, 0};
            pk[h][0] = __builtin_bit_cast(unsigned, __builtin_elementwise_max(__builtin_bit_cast(s16x2, pk[h][0]), z));
            pk[h][1] = __builtin_bit_cast(unsigned, __builtin_elementwise_max(__builtin_bit_cast(s16x2, pk[h][1]), z));
          }
        }
        auto sx = __builtin_amdgcn_permlane16_swap(pk[0][0], pk[1][0], false, false);
        auto sy = __builtin_amdgcn_permlane16_swap(pk[0][1], pk[1][1], false, false);
        u32x4 v = {sx[0], sy[0], sx[1], sy[1]};
        int row = m0 + wm * 64 + (((mi + 2 * wn) & 3) * 16) + r16;
        if constexpr (BT) {
          if (n < 1024) {
            u16* dst = n < 512 ? AB + (size_t)row * D_ + n : AB + (size_t)(S_ + row) * D_ + (n - 512);
            *(u32x4*)dst = v;
          }
        } else {
          if (n < F_) *(u32x4*)(H + (size_t)row * HLD + n) = v;
        }
      }
    }
  }
}

__global__ __launch_bounds__(512, 1) void k1_kernel(const u16* __restrict__ X, const u16* __restrict__ Hr,
                                                    const u16* __restrict__ Hi, const u16* __restrict__ W1,
                                                    const u16* __restrict__ Cr, const u16* __restrict__ Ci,
                                                    const u16* __restrict__ W2, u16* __restrict__ AB,
                                                    u16* __restrict__ H, char* __restrict__ xch) {
  extern __shared__ __attribute__((aligned(1024))) char smem[];
  const int bid = blockIdx.x;
  char* dbg = xch + (17u << 20);
  STAMP(dbg, 0);
  const int xcd = bid & 7, idx = bid >> 3;
  const int mt = (xcd & 1) * 8 + (idx & 7);
  const int nt = (xcd >> 1) * 4 + (idx >> 3);
  if (nt < 6) k1_body<true>(smem, X, Hr, Hi, W1, Cr, Ci, W2, AB, H, xch, mt * 128, nt * 192, dbg);
  else k1_body<false>(smem, X, Hr, Hi, W1, Cr, Ci, W2, AB, H, xch, mt * 128, (nt - 6) * 192, dbg);
}

constexpr int K2_STAGE = 32768;
constexpr int K2_LDS = 4 * K2_STAGE;
constexpr int K2_NS = 23;

__global__ __launch_bounds__(512, 1) void k2_kernel(const u16* __restrict__ Cr, const u16* __restrict__ Ci,
                                                    const u16* __restrict__ H, const u16* __restrict__ AB,
                                                    const u16* __restrict__ W2, const u16* __restrict__ X,
                                                    u16* __restrict__ out, char* __restrict__ xch) {
  extern __shared__ __attribute__((aligned(1024))) char smem[];
  const int tid = threadIdx.x, lane = tid & 63;
  const int wv = __builtin_amdgcn_readfirstlane(tid >> 6);
  const int w = wv & 3;
  const int wm = w >> 1, wn = w & 1;
  const int bid = blockIdx.x;
  const int kq = bid & 3, mh = (bid >> 2) & 1, idx = bid >> 3;
  const int mt = mh * 8 + (idx >> 2), nt = idx & 3;
  const int m0 = mt * 128, n0 = nt * 128;
  const int tile = mt * 4 + nt;
  char* dbg = xch + (16u << 20);
  STAMP(dbg, 0);

  if (wv >= 4) {
    const int pw = w;
    const int rj = (nt + (idx >> 2)) & 3;
    unsigned aoffg[4], boffg[4];
#pragma unroll
    for (int i = 0; i < 4; ++i) {
      int r = (pw * 4 + i) * 8 + (lane >> 3);
      int grow = m0 + (r & 64) + ((((r >> 4) + kq) & 3) << 4) + (r & 15);
      int ch = (lane & 7) ^ (r & 7);
      aoffg[i] = (unsigned)(grow * 4096 + ch * 16);
      int k = (pw * 4 + i) * 4 + (lane >> 4);
      int f = (k & 3) | (((k >> 3) & 1) << 2);
      int lc = (lane & 15) ^ (f << 1);
      boffg[i] = (unsigned)(k * 1024 + n0 * 2 + lc * 16);
    }
    const unsigned lbase = (unsigned)(size_t)(LAS char*)smem + pw * 4096;
    auto stage = [&](int i) {
      const char *abase, *bbase;
      int s;
      if (i < 20) s = (i & ~3) | ((i + rj) & 3);
      else { s = i - 20 + rj; s = 20 + (s >= 3 ? s - 3 : s); }
      if (s < 16) {
        abase = (const char*)(s < 8 ? Cr : Ci) + kq * 1024 + (s & 7) * 128;
        bbase = (const char*)AB + (size_t)((s & 8) * 256 + kq * 512 + (s & 7) * 64) * 1024;
      } else {
        abase = (const char*)H + kq * 896 + (s - 16) * 128;
        bbase = (const char*)W2 + (size_t)(kq * 448 + (s - 16) * 64) * 1024;
      }
      abase = uni(abase); bbase = uni(bbase);
      unsigned base = lbase + (i & 3) * K2_STAGE;
#pragma unroll
      for (int j = 0; j < 4; ++j) glds16s(boffg[j], bbase, base + 16384 + j * 1024);
#pragma unroll
      for (int j = 0; j < 4; ++j) glds16s(aoffg[j], abase, base + j * 1024);
    };
    stage(0); stage(1); stage(2);
    WAIT_V(16);
    RAWBAR();
    stage(3);
    for (int i = 1; i < K2_NS; ++i) {
      if (i + 2 < K2_NS) WAIT_V(16);
      else if (i + 1 < K2_NS) WAIT_V(8);
      else WAIT_V(0);
      RAWBAR();
      if (i + 3 < K2_NS) stage(i + 3);
    }
    {
      const int g = lane >> 4, r16 = lane & 15;
      const int orow = m0 + wm * 64 + kq * 16 + r16;
      u32x2 xres[4];
#pragma unroll
      for (int ni = 0; ni < 4; ++ni)
        xres[ni] = *(const u32x2*)(X + (size_t)orow * D_ + n0 + wn * 64 + ni * 16 + 4 * g);
      __amdgpu_buffer_rsrc_t xr = __builtin_amdgcn_make_buffer_rsrc((void*)xch, 0, XCH_BYTES + FLAG_BYTES, 0x00020000);
      const unsigned foff = XCH_BYTES + (unsigned)(((tile * 4 + kq) * 4 + w) * 32) + (lane & 7) * 4;
      const unsigned off = (unsigned)(((tile * 4 + kq) * 3 * 4 + w) * 2048 + lane * 16);
      RAWBAR();
      STAMP(dbg, 3);
      u32x4 pv[3][2];
      unsigned have = 0;
#pragma unroll
      for (int j = 3; j >= 1; --j) {
        for (unsigned spins = 0; !(have & (1u << (j - 1))) && spins < (1u << 22); ++spins) {
          asm volatile("" ::: "memory");
          unsigned f = __builtin_amdgcn_raw_buffer_load_b32(xr, foff, 0, 16);
          have = (unsigned)__builtin_amdgcn_ballot_w64(f == 0x5eedu) & 0x7u;
          have = __builtin_amdgcn_readfirstlane(have);
          if (!(have & (1u << (j - 1)))) __builtin_amdgcn_s_sleep(1);
        }
        asm volatile("" ::: "memory");
        pv[j - 1][0] = __builtin_amdgcn_raw_buffer_load_b128(xr, off + (j - 1) * 8192, 0, 16);
        pv[j - 1][1] = __builtin_amdgcn_raw_buffer_load_b128(xr, off + (j - 1) * 8192 + 1024, 0, 16);
      }
      STAMP(dbg, 4);
#pragma unroll
      for (int ni = 0; ni < 4; ++ni) {
        f32x4 v = *(const LAS f32x4*)(smem + (w * 4 + ni) * 1024 + lane * 16);
#pragma unroll
        for (int j = 0; j < 3; ++j) {
          unsigned p0 = pv[j][ni >> 1][(ni & 1) * 2], p1 = pv[j][ni >> 1][(ni & 1) * 2 + 1];
          v[0] += bf2f(p0 & 0xffffu);
          v[1] += bf2f(p0 >> 16);
          v[2] += bf2f(p1 & 0xffffu);
          v[3] += bf2f(p1 >> 16);
        }
        v[0] += bf2f(xres[ni][0] & 0xffffu);
        v[1] += bf2f(xres[ni][0] >> 16);
        v[2] += bf2f(xres[ni][1] & 0xffffu);
        v[3] += bf2f(xres[ni][1] >> 16);
        *(u32x2*)(out + (size_t)orow * D_ + n0 + wn * 64 + ni * 16 + 4 * g) = (u32x2){pack2(v[0], v[1]), pack2(v[2], v[3])};
      }
    }
    return;
  }

  const int g = lane >> 4, r16 = lane & 15;
  const int aoff = (wm * 64 + r16) * 128 + ((g ^ (r16 & 7)) * 16);
  int boff[4];
  {
    int q = r16 >> 2, p = lane & 3;
    int k = 8 * g + q;
    int f = q | ((g & 1) << 2);
#pragma unroll
    for (int ni = 0; ni < 4; ++ni) {
      int lc = wn * 8 + ni * 2 + (p >> 1);
      boff[ni] = 16384 + k * 256 + ((lc ^ (f << 1)) * 16) + 8 * (p & 1);
    }
  }
  f32x4 acc[4][4];
#pragma unroll
  for (int ni = 0; ni < 4; ++ni)
#pragma unroll
    for (int mi = 0; mi < 4; ++mi) acc[ni][mi] = (f32x4){0.f, 0.f, 0.f, 0.f};

  s16x8 a0[4], b0[4], a1[4], b1[4];
#define K2_READ(AF, BF, BASE, KK)                                                  \
  do {                                                                             \
    _Pragma("unroll") for (int ni = 0; ni < 4; ++ni) {                             \
      AF[ni] = lds_b128((BASE) + (aoff ^ ((KK) * 64)) + ni * 2048);                \
      s16x4 lo = lds_tr((BASE) + boff[ni] + (KK) * 8192);                          \
      s16x4 hi = lds_tr((BASE) + boff[ni] + (KK) * 8192 + 1024);                   \
      BF[ni] = cat(lo, hi);                                                        \
    }                                                                              \
  } while (0)
#define K2_MMA(AF, BF)                                                             \
  do {                                                                             \
    _Pragma("unroll") for (int ni = 0; ni < 4; ++ni)                               \
      _Pragma("unroll") for (int mi = 0; mi < 4; ++mi)                             \
        acc[ni][mi] = mfma16(BF[ni], AF[mi], acc[ni][mi]);                         \
  } while (0)
#define K2_SCHED()                                                                 \
  do {                                                                             \
    _Pragma("unroll") for (int q_ = 0; q_ < 12; ++q_) {                            \
      __builtin_amdgcn_sched_group_barrier(0x008, 1, 0);                           \
      __builtin_amdgcn_sched_group_barrier(0x100, 1, 0);                           \
    }                                                                              \
    __builtin_amdgcn_sched_group_barrier(0x008, 4, 0);                             \
  } while (0)

  BARRIER();
  STAMP(dbg, 1);
  K2_READ(a0, b0, smem, 0);
  for (int i = 0; i < K2_NS - 1; ++i) {
    const char* base = smem + (i & 3) * K2_STAGE;
    const char* nbase = smem + ((i + 1) & 3) * K2_STAGE;
    K2_READ(a1, b1, base, 1);
    K2_MMA(a0, b0);
    K2_SCHED();
    __builtin_amdgcn_sched_barrier(0);
    BARRIER();
    K2_READ(a0, b0, nbase, 0);
    K2_MMA(a1, b1);
    K2_SCHED();
    __builtin_amdgcn_sched_barrier(0);
  }
  {
    const char* base = smem + ((K2_NS - 1) & 3) * K2_STAGE;
    K2_READ(a1, b1, base, 1);
    K2_MMA(a0, b0);
    K2_MMA(a1, b1);
  }
  STAMP(dbg, 2);

  __amdgpu_buffer_rsrc_t xr = __builtin_amdgcn_make_buffer_rsrc((void*)xch, 0, XCH_BYTES + FLAG_BYTES, 0x00020000);
#pragma unroll
  for (int mi = 1; mi < 4; ++mi) {
    int dest = (kq + mi) & 3;
    unsigned off = (unsigned)((((tile * 4 + dest) * 3 + (3 - mi)) * 4 + w) * 2048 + lane * 16);
#pragma unroll
    for (int hf = 0; hf < 2; ++hf) {
      u32x4 v;
#pragma unroll
      for (int q = 0; q < 2; ++q) {
        f32x4 a = acc[hf * 2 + q][mi];
        v[q * 2] = pack2(a[0], a[1]);
        v[q * 2 + 1] = pack2(a[2], a[3]);
      }
      __builtin_amdgcn_raw_buffer_store_b128(v, xr, off + hf * 1024, 0, 16);
    }
  }
#pragma unroll
  for (int ni = 0; ni < 4; ++ni) *(LAS f32x4*)(smem + (w * 4 + ni) * 1024 + lane * 16) = acc[ni][0];
  BARRIER();
#define FLAG(mi, n) do { WAIT_V(n); if (lane == 0) __builtin_amdgcn_raw_buffer_store_b32(0x5eedu, xr, XCH_BYTES + (unsigned)(((tile * 4 + ((kq + mi) & 3)) * 4 + w) * 32 + (3 - mi) * 4), 0, 16); } while (0)
  FLAG(1, 4); FLAG(2, 3); FLAG(3, 2);
}

extern "C" void kernel_launch(void* const* d_in, const int* in_sizes, int n_in, void* d_out, int out_size,
                              void* d_ws, size_t ws_size, hipStream_t stream) {
  const u16* X = (const u16*)d_in[0];
  const u16* Cr = (const u16*)d_in[1];
  const u16* Ci = (const u16*)d_in[2];
  const u16* Hr = (const u16*)d_in[3];
  const u16* Hi = (const u16*)d_in[4];
  const u16* W1 = (const u16*)d_in[6];
  const u16* W2 = (const u16*)d_in[7];
  char* ws = (char*)d_ws;
  u16* AB = (u16*)(ws);
  u16* H = (u16*)(ws + 4194304);
  char* xch = ws + 4194304 + 8388608;
  (void)hipFuncSetAttribute((const void*)k1_kernel, hipFuncAttributeMaxDynamicSharedMemorySize, K1_LDS);
  (void)hipFuncSetAttribute((const void*)k2_kernel, hipFuncAttributeMaxDynamicSharedMemorySize, K2_LDS);
  k1_kernel<<<256, 512, K1_LDS, stream>>>(X, Hr, Hi, W1, Cr, Ci, W2, AB, H, xch);
  k2_kernel<<<256, 512, K2_LDS, stream>>>(Cr, Ci, H, AB, W2, X, (u16*)d_out, xch);
}
```

```cpp
#include <hip/hip_runtime.h>
#include <stdint.h>

typedef short s16x8 __attribute__((ext_vector_type(8)));
typedef short s16x4 __attribute__((ext_vector_type(4)));
typedef __bf16 bf16x2 __attribute__((ext_vector_type(2)));
typedef float f32x4 __attribute__((ext_vector_type(4)));
typedef unsigned u32x4 __attribute__((ext_vector_type(4)));
typedef unsigned u32x2 __attribute__((ext_vector_type(2)));
typedef unsigned short u16;
#define LAS __attribute__((address_space(3)))
#define GAS __attribute__((address_space(1)))

constexpr int S_ = 2048, D_ = 512, F_ = 1792;
constexpr int HLD = 2048;

#define STAMP(buf, slot) do {} while (0)
#define WAIT_V(n) asm volatile("s_waitcnt vmcnt(%0)" ::"n"(n) : "memory")
#define WAIT_L0() asm volatile("s_waitcnt lgkmcnt(0)" ::: "memory")
#define BARRIER() do { WAIT_L0(); __builtin_amdgcn_s_barrier(); asm volatile("" ::: "memory"); } while (0)
#define RAWBAR() do { asm volatile("" ::: "memory"); __builtin_amdgcn_s_barrier(); asm volatile("" ::: "memory"); } while (0)

__device__ __forceinline__ float bf2f(unsigned h) { return __uint_as_float(h << 16); }
__device__ __forceinline__ float rbf(float f) {
  __bf16 b = (__bf16)f;
  return (float)b;
}
__device__ __forceinline__ unsigned pack2(float lo, float hi) {
  bf16x2 v;
  v[0] = (__bf16)lo;
  v[1] = (__bf16)hi;
  return __builtin_bit_cast(unsigned, v);
}
__device__ __forceinline__ void glds16(const void* g, unsigned lds) {
  asm volatile("s_mov_b32 m0, %1\n\ts_nop 0\n\tglobal_load_lds_dwordx4 %0, off" ::"v"(g), "s"(lds) : "memory");
}
__device__ __forceinline__ const char* uni(const char* p) {
  unsigned long long v = (unsigned long long)p;
  unsigned lo = __builtin_amdgcn_readfirstlane((unsigned)v), hi = __builtin_amdgcn_readfirstlane((unsigned)(v >> 32));
  return (const char*)(((unsigned long long)hi << 32) | lo);
}
__device__ __forceinline__ void glds16s(unsigned voff, const void* sbase, unsigned lds) {
  asm volatile("s_mov_b32 m0, %2\n\ts_nop 0\n\tglobal_load_lds_dwordx4 %0, %1" ::"v"(voff), "s"(sbase), "s"(lds) : "memory");
}
#define touch(g) asm volatile("global_load_dword %0, %1, off" : "+v"(sink) : "v"((const void*)(g)) : "memory")
#define TOUCH_END() do { WAIT_V(0); asm volatile("" ::"v"(sink)); } while (0)
__device__ __forceinline__ s16x8 lds_b128(const char* p) { return *(const LAS s16x8*)p; }
__device__ __forceinline__ s16x4 lds_tr(const char* p) {
  return __builtin_amdgcn_ds_read_tr16_b64_v4i16((LAS s16x4*)p);
}
__device__ __forceinline__ s16x8 cat(s16x4 a, s16x4 b) {
  return __builtin_shufflevector(a, b, 0, 1, 2, 3, 4, 5, 6, 7);
}
__device__ __forceinline__ f32x4 mfma16(s16x8 a, s16x8 b, f32x4 c) {
  return __builtin_amdgcn_mfma_f32_16x16x32_bf16(a, b, c, 0, 0, 0);
}
__device__ __forceinline__ float dot2(unsigned a, unsigned b, float c) {
  return __builtin_amdgcn_fdot2_f32_bf16(__builtin_bit_cast(bf16x2, a), __builtin_bit_cast(bf16x2, b), c, false);
}

constexpr unsigned XCH_BYTES = 64u * 4u * 3u * 4u * 2048u;
constexpr unsigned FLAG_BYTES = 32768u;

constexpr int K1_STAGE = 40960;
constexpr int K1_LDS = 4 * K1_STAGE;

template <bool BT>
__device__ __forceinline__ void k1_body(char* smem, const u16* __restrict__ X, const u16* __restrict__ Hr,
                                        const u16* __restrict__ Hi, const u16* __restrict__ W1,
                                        const u16* __restrict__ Cr, const u16* __restrict__ Ci, const u16* __restrict__ W2,
                                        u16* __restrict__ AB, u16* __restrict__ H, char* __restrict__ xch,
                                        int m0, int n0, char* dbg) {
  const int tid = threadIdx.x, lane = tid & 63;
  const int wv = __builtin_amdgcn_readfirstlane(tid >> 6);
  const int w = wv & 3;
  const int wm = w >> 1, wn = w & 1;

  if (wv >= 4) {
    const int pw = wv - 4;
    const char* aptr[4];
#pragma unroll
    for (int i = 0; i < 4; ++i) {
      int row = (pw * 4 + i) * 8 + (lane >> 3);
      int ch = (lane & 7) ^ (row & 7);
      aptr[i] = (const char*)(X + (size_t)(m0 + row) * D_) + ch * 16;
    }
    const char* bptr[6];
#pragma unroll
    for (int i = 0; i < 6; ++i) {
      int u = (pw * 6 + i) * 64 + lane;
      if constexpr (BT) {
        int row = u >> 3;
        int ch = (u & 7) ^ (row & 7);
        int n = n0 + row;
        n = n > 1023 ? 1023 : n;
        const u16* src = n < 512 ? Hr + (size_t)n * D_ : Hi + (size_t)(n - 512) * D_;
        bptr[i] = (const char*)src + ch * 16;
      } else {
        int k = u / 24, pc = u - k * 24;
        int f = ((k >> 1) & 1) | (((k >> 3) & 1) << 1);
        int lc = pc ^ (f << 1);
        int col = n0 + lc * 8;
        col = col > F_ - 8 ? F_ - 8 : col;
        bptr[i] = (const char*)(W1 + (size_t)k * F_ + col);
      }
    }
    constexpr int BSTEP = BT ? 128 : 64 * F_ * 2;
    const unsigned lbase = (unsigned)(size_t)(LAS char*)smem;
    const int rj = ((blockIdx.x >> 3) + (blockIdx.x >> 6)) & 3;
    auto stage = [&](int s) {
      unsigned base = lbase + (s & 3) * K1_STAGE;
      int ks_ = (s & ~3) | ((s + rj) & 3);
      if (s == 0) ks_ = 0;
      else if (s < 4) { int t = s - 1 + rj; t = t >= 3 ? t - 3 : t; ks_ = 1 + t; }
#pragma unroll
      for (int i = 0; i < 4; ++i) glds16(aptr[i] + ks_ * 128, base + (pw * 4 + i) * 1024);
#pragma unroll
      for (int i = 0; i < 6; ++i) glds16(bptr[i] + (size_t)ks_ * BSTEP, base + 16384 + (pw * 6 + i) * 1024);
    };
    unsigned sink = 0;
    stage(0); stage(1);
    WAIT_V(10);
    RAWBAR();
    stage(2);
    WAIT_V(10);
    RAWBAR();
    stage(3); stage(4);
    for (int s = 2; s < 5; ++s) {
      WAIT_V(20);
      RAWBAR();
      stage(s + 3);
    }
    auto extras = [&]() {
      const int kq = blockIdx.x & 3, mh = (blockIdx.x >> 2) & 1, idx = blockIdx.x >> 3;
#pragma unroll
      for (int i = 0; i < 2; ++i) {
        const u16* src = (pw & 1) ? Ci : Cr;
        int ln = ((pw >> 1) * 2 + i) * 64 + lane;
        touch((const char*)src + (size_t)(mh * 1024 + idx * 32 + (ln >> 3)) * 4096 + kq * 1024 + (ln & 7) * 128);
      }
      {
        int ln = pw * 28 + (lane < 28 ? lane : 27);
        touch((const char*)W2 + (size_t)kq * 458752 + (idx * 112 + ln) * 128);
      }
      __amdgpu_buffer_rsrc_t xr = __builtin_amdgcn_make_buffer_rsrc((void*)xch, 0, XCH_BYTES + FLAG_BYTES, 0x00020000);
      if (lane < 8) __builtin_amdgcn_raw_buffer_store_b32(0u, xr, XCH_BYTES + blockIdx.x * 128u + pw * 32u + lane * 4u, 0, 16);
    };
    extras();
    WAIT_V(24);
    RAWBAR();
    WAIT_V(14);
    RAWBAR();
    WAIT_V(4);
    RAWBAR();
    RAWBAR();
    TOUCH_END();
    return;
  }

  const int g = lane >> 4, r16 = lane & 15;
  int aoff[4];
#pragma unroll
  for (int mi = 0; mi < 4; ++mi) {
    int row = wm * 64 + (((mi + 2 * wn) & 3) * 16) + r16;
    aoff[mi] = row * 128 + ((g ^ (row & 7)) * 16);
  }
  int boff[6];
#pragma unroll
  for (int ni = 0; ni < 6; ++ni) {
    int pb = ni + 3 * wm;
    pb = pb >= 6 ? pb - 6 : pb;
    int nb = wn * 6 + pb;
    if constexpr (BT) {
      int row = nb * 16 + r16;
      boff[ni] = 16384 + row * 128 + ((g ^ (row & 7)) * 16);
    } else {
      int q = r16 >> 2, p = lane & 3;
      int k = 8 * g + q;
      int f = ((k >> 1) & 1) | (((k >> 3) & 1) << 1);
      int lc = nb * 2 + (p >> 1);
      boff[ni] = 16384 + k * 384 + ((lc ^ (f << 1)) * 16) + 8 * (p & 1);
    }
  }

  f32x4 acc[6][4];
#pragma unroll
  for (int ni = 0; ni < 6; ++ni)
#pragma unroll
    for (int mi = 0; mi < 4; ++mi) acc[ni][mi] = (f32x4){0.f, 0.f, 0.f, 0.f};
  f32x4 rs[2], rq[2], cs[3];
#pragma unroll
  for (int i = 0; i < 2; ++i) { rs[i] = (f32x4){0.f, 0.f, 0.f, 0.f}; rq[i] = (f32x4){0.f, 0.f, 0.f, 0.f}; }
#pragma unroll
  for (int i = 0; i < 3; ++i) cs[i] = (f32x4){0.f, 0.f, 0.f, 0.f};
  const s16x8 ones = {0x3f80, 0x3f80, 0x3f80, 0x3f80, 0x3f80, 0x3f80, 0x3f80, 0x3f80};
  s16x8 a0[4], b0[6], a1[4], b1[6];

#define K1_READ(AF, BF, BASE, KK)                                                        \
  do {                                                                                   \
    _Pragma("unroll") for (int ni = 0; ni < 6; ++ni) {                                   \
      if (ni < 4) AF[ni] = lds_b128((BASE) + (aoff[ni] ^ ((KK) * 64)));                  \
      if constexpr (BT) BF[ni] = lds_b128((BASE) + (boff[ni] ^ ((KK) * 64)));            \
      else {                                                                             \
        s16x4 lo = lds_tr((BASE) + boff[ni] + (KK) * 32 * 384);                          \
        s16x4 hi = lds_tr((BASE) + boff[ni] + (KK) * 32 * 384 + 4 * 384);                \
        BF[ni] = cat(lo, hi);                                                            \
      }                                                                                  \
    }                                                                                    \
  } while (0)
#define K1_MMA(AF, BF)                                                                   \
  do {                                                                                   \
    _Pragma("unroll") for (int ni = 0; ni < 6; ++ni)                                     \
      _Pragma("unroll") for (int mi = 0; mi < 4; ++mi)                                   \
        acc[ni][mi] = mfma16(BF[ni], AF[mi], acc[ni][mi]);                               \
    _Pragma("unroll") for (int mi = 0; mi < 2; ++mi) {                                   \
      rs[mi] = mfma16(ones, AF[mi], rs[mi]);                                             \
      rq[mi] = mfma16(AF[mi], AF[mi], rq[mi]);                                           \
    }                                                                                    \
    _Pragma("unroll") for (int ni = 0; ni < 3; ++ni) cs[ni] = mfma16(BF[ni], ones, cs[ni]); \
  } while (0)
#define K1_SCHED()                                                                       \
  do {                                                                                   \
    _Pragma("unroll") for (int q_ = 0; q_ < 16; ++q_) {                                  \
      __builtin_amdgcn_sched_group_barrier(0x008, 1, 0);                                 \
      __builtin_amdgcn_sched_group_barrier(0x100, 1, 0);                                 \
    }                                                                                    \
    __builtin_amdgcn_sched_group_barrier(0x008, 15, 0);                                  \
  } while (0)

  BARRIER();
  STAMP(dbg, 1);
  K1_READ(a0, b0, smem, 0);
  for (int s = 0; s < 7; ++s) {
    const char* base = smem + (s & 3) * K1_STAGE;
    const char* nbase = smem + ((s + 1) & 3) * K1_STAGE;
    K1_READ(a1, b1, base, 1);
    K1_MMA(a0, b0);
    K1_SCHED();
    __builtin_amdgcn_sched_barrier(0);
    BARRIER();
    K1_READ(a0, b0, nbase, 0);
    K1_MMA(a1, b1);
    K1_SCHED();
    __builtin_amdgcn_sched_barrier(0);
  }
  {
    const char* base = smem + 3 * K1_STAGE;
    K1_READ(a1, b1, base, 1);
    K1_MMA(a0, b0);
    K1_MMA(a1, b1);
  }
  STAMP(dbg, 2);
  float* st_mean = (float*)smem;
  float* st_rinv = (float*)smem + 128;
  float* st_cs = (float*)smem + 256;
#pragma unroll
  for (int mi = 0; mi < 2; ++mi) {
    float b = (r16 & 2) ? ((r16 & 1) ? rq[mi][3] : rq[mi][2]) : ((r16 & 1) ? rq[mi][1] : rq[mi][0]);
    float mean = rs[mi][0] * (1.0f / D_);
    float var = b * (1.0f / D_) - mean * mean;
    var = var < 0.f ? 0.f : var;
    float mb = rbf(mean);
    float sb = rbf(sqrtf(rbf(rbf(var) + 1e-5f)));
    int row = wm * 64 + (((mi + 2 * wn) & 3) * 16) + r16;
    if (g == (r16 >> 2)) { st_mean[row] = mb; st_rinv[row] = 1.0f / sb; }
  }
#pragma unroll
  for (int ni = 0; ni < 3; ++ni) {
    int nb = wn * 6 + ni + 3 * wm;
    if (r16 == 0) *(f32x4*)(st_cs + nb * 16 + 4 * g) = cs[ni];
  }
  BARRIER();
  STAMP(dbg, 3);
  {
    typedef float f32x2 __attribute__((ext_vector_type(2)));
    typedef short s16x2 __attribute__((ext_vector_type(2)));
    float nmean[4], rinv[4];
#pragma unroll
    for (int mi = 0; mi < 4; ++mi) {
      int rl = wm * 64 + (((mi + 2 * wn) & 3) * 16) + r16;
      nmean[mi] = -st_mean[rl];
      rinv[mi] = st_rinv[rl];
    }
#pragma unroll
    for (int np = 0; np < 3; ++np) {
      int pb0 = 2 * np + 3 * wm, pb1 = 2 * np + 1 + 3 * wm;
      pb0 = pb0 >= 6 ? pb0 - 6 : pb0;
      pb1 = pb1 >= 6 ? pb1 - 6 : pb1;
      const int nb0 = (wn * 6 + pb0) * 16, nb1 = (wn * 6 + pb1) * 16;
      f32x4 c0 = *(const f32x4*)(st_cs + nb0 + 4 * g), c1 = *(const f32x4*)(st_cs + nb1 + 4 * g);
      const int n = n0 + ((g & 1) ? nb1 : nb0) + (g >> 1) * 8;
      float sg0 = 1.f, sg1 = 1.f;
      if constexpr (BT) { sg0 = n0 + nb0 >= 512 ? -1.f : 1.f; sg1 = n0 + nb1 >= 512 ? -1.f : 1.f; }
#pragma unroll
      for (int mi = 0; mi < 4; ++mi) {
        unsigned pk[2][2];
#pragma unroll
        for (int h = 0; h < 2; ++h) {
          f32x4 a = acc[2 * np + h][mi];
          f32x4 cc = h ? c1 : c0;
          float r = BT ? rinv[mi] * (h ? sg1 : sg0) : rinv[mi];
          f32x2 m2 = {nmean[mi], nmean[mi]}, r2 = {r, r};
          f32x2 lo = ((f32x2){cc[0], cc[1]} * m2 + (f32x2){a[0], a[1]}) * r2;
          f32x2 hi = ((f32x2){cc[2], cc[3]} * m2 + (f32x2){a[2], a[3]}) * r2;
          pk[h][0] = pack2(lo[0], lo[1]);
          pk[h][1] = pack2(hi[0], hi[1]);
          if constexpr (!BT) {
            const s16x2 z = {0, 0};
            pk[h][0] = __builtin_bit_cast(unsigned, __builtin_elementwise_max(__builtin_bit_cast(s16x2, pk[h][0]), z));
            pk[h][1] = __builtin_bit_cast(unsigned, __builtin_elementwise_max(__builtin_bit_cast(s16x2, pk[h][1]), z));
          }
        }
        auto sx = __builtin_amdgcn_permlane16_swap(pk[0][0], pk[1][0], false, false);
        auto sy = __builtin_amdgcn_permlane16_swap(pk[0][1], pk[1][1], false, false);
        u32x4 v = {sx[0], sy[0], sx[1], sy[1]};
        int row = m0 + wm * 64 + (((mi + 2 * wn) & 3) * 16) + r16;
        if constexpr (BT) {
          if (n < 1024) {
            u16* dst = n < 512 ? AB + (size_t)row * D_ + n : AB + (size_t)(S_ + row) * D_ + (n - 512);
            *(u32x4*)dst = v;
          }
        } else {
          if (n < F_) *(u32x4*)(H + (size_t)row * HLD + n) = v;
        }
      }
    }
  }
}

__global__ __launch_bounds__(512, 1) void k1_kernel(const u16* __restrict__ X, const u16* __restrict__ Hr,
                                                    const u16* __restrict__ Hi, const u16* __restrict__ W1,
                                                    const u16* __restrict__ Cr, const u16* __restrict__ Ci,
                                                    const u16* __restrict__ W2, u16* __restrict__ AB,
                                                    u16* __restrict__ H, char* __restrict__ xch) {
  extern __shared__ __attribute__((aligned(1024))) char smem[];
  const int bid = blockIdx.x;
  char* dbg = xch + (17u << 20);
  STAMP(dbg, 0);
  const int xcd = bid & 7, idx = bid >> 3;
  const int mt = (xcd & 1) * 8 + (idx & 7);
  const int nt = (xcd >> 1) * 4 + (idx >> 3);
  if (nt < 6) k1_body<true>(smem, X, Hr, Hi, W1, Cr, Ci, W2, AB, H, xch, mt * 128, nt * 192, dbg);
  else k1_body<false>(smem, X, Hr, Hi, W1, Cr, Ci, W2, AB, H, xch, mt * 128, (nt - 6) * 192, dbg);
}

constexpr int K2_STAGE = 32768;
constexpr int K2_LDS = 4 * K2_STAGE;
constexpr int K2_NS = 23;

__global__ __launch_bounds__(512, 1) void k2_kernel(const u16* __restrict__ Cr, const u16* __restrict__ Ci,
                                                    const u16* __restrict__ H, const u16* __restrict__ AB,
                                                    const u16* __restrict__ W2, const u16* __restrict__ X,
                                                    u16* __restrict__ out, char* __restrict__ xch) {
  extern __shared__ __attribute__((aligned(1024))) char smem[];
  const int tid = threadIdx.x, lane = tid & 63;
  const int wv = __builtin_amdgcn_readfirstlane(tid >> 6);
  const int w = wv & 3;
  const int wm = w >> 1, wn = w & 1;
  const int bid = blockIdx.x;
  const int kq = bid & 3, mh = (bid >> 2) & 1, idx = bid >> 3;
  const int mt = mh * 8 + (idx >> 2), nt = idx & 3;
  const int m0 = mt * 128, n0 = nt * 128;
  const int tile = mt * 4 + nt;
  char* dbg = xch + (16u << 20);
  STAMP(dbg, 0);

  if (wv >= 4) {
    const int pw = w;
    const int rj = (nt + (idx >> 2)) & 3;
    unsigned aoffg[4], boffg[4];
#pragma unroll
    for (int i = 0; i < 4; ++i) {
      int r = (pw * 4 + i) * 8 + (lane >> 3);
      int grow = m0 + (r & 64) + ((((r >> 4) + kq) & 3) << 4) + (r & 15);
      int ch = (lane & 7) ^ (r & 7);
      aoffg[i] = (unsigned)(grow * 4096 + ch * 16);
      int k = (pw * 4 + i) * 4 + (lane >> 4);
      int f = (k & 3) | (((k >> 3) & 1) << 2);
      int lc = (lane & 15) ^ (f << 1);
      boffg[i] = (unsigned)(k * 1024 + n0 * 2 + lc * 16);
    }
    const unsigned lbase = (unsigned)(size_t)(LAS char*)smem + pw * 4096;
    auto stage = [&](int i) {
      const char *abase, *bbase;
      int s;
      if (i < 20) s = (i & ~3) | ((i + rj) & 3);
      else { s = i - 20 + rj; s = 20 + (s >= 3 ? s - 3 : s); }
      if (s < 16) {
        abase = (const char*)(s < 8 ? Cr : Ci) + kq * 1024 + (s & 7) * 128;
        bbase = (const char*)AB + (size_t)((s & 8) * 256 + kq * 512 + (s & 7) * 64) * 1024;
      } else {
        abase = (const char*)H + kq * 896 + (s - 16) * 128;
        bbase = (const char*)W2 + (size_t)(kq * 448 + (s - 16) * 64) * 1024;
      }
      abase = uni(abase); bbase = uni(bbase);
      unsigned base = lbase + (i & 3) * K2_STAGE;
#pragma unroll
      for (int j = 0; j < 4; ++j) glds16s(boffg[j], bbase, base + 16384 + j * 1024);
#pragma unroll
      for (int j = 0; j < 4; ++j) glds16s(aoffg[j], abase, base + j * 1024);
    };
    stage(0); stage(1);
    WAIT_V(8);
    RAWBAR();
    stage(2);
    WAIT_V(8);
    RAWBAR();
    stage(3); stage(4);
    for (int i = 2; i < K2_NS; ++i) {
      if (i + 2 < K2_NS) WAIT_V(16);
      else if (i + 1 < K2_NS) WAIT_V(8);
      else WAIT_V(0);
      RAWBAR();
      if (i + 3 < K2_NS) stage(i + 3);
    }
    {
      const int g = lane >> 4, r16 = lane & 15;
      const int orow = m0 + wm * 64 + kq * 16 + r16;
      u32x2 xres[4];
#pragma unroll
      for (int ni = 0; ni < 4; ++ni)
        xres[ni] = *(const u32x2*)(X + (size_t)orow * D_ + n0 + wn * 64 + ni * 16 + 4 * g);
      __amdgpu_buffer_rsrc_t xr = __builtin_amdgcn_make_buffer_rsrc((void*)xch, 0, XCH_BYTES + FLAG_BYTES, 0x00020000);
      const unsigned foff = XCH_BYTES + (unsigned)(((tile * 4 + kq) * 4 + w) * 32) + (lane & 7) * 4;
      const unsigned off = (unsigned)(((tile * 4 + kq) * 3 * 4 + w) * 2048 + lane * 16);
      RAWBAR();
      STAMP(dbg, 3);
      u32x4 pv[3][2];
      unsigned have = 0;
#pragma unroll
      for (int j = 3; j >= 1; --j) {
        for (unsigned spins = 0; !(have & (1u << (j - 1))) && spins < (1u << 22); ++spins) {
          asm volatile("" ::: "memory");
          unsigned f = __builtin_amdgcn_raw_buffer_load_b32(xr, foff, 0, 16);
          have = (unsigned)__builtin_amdgcn_ballot_w64(f == 0x5eedu) & 0x7u;
          have = __builtin_amdgcn_readfirstlane(have);
          if (!(have & (1u << (j - 1)))) __builtin_amdgcn_s_sleep(1);
        }
        asm volatile("" ::: "memory");
        pv[j - 1][0] = __builtin_amdgcn_raw_buffer_load_b128(xr, off + (j - 1) * 8192, 0, 16);
        pv[j - 1][1] = __builtin_amdgcn_raw_buffer_load_b128(xr, off + (j - 1) * 8192 + 1024, 0, 16);
      }
      STAMP(dbg, 4);
#pragma unroll
      for (int ni = 0; ni < 4; ++ni) {
        f32x4 v = *(const LAS f32x4*)(smem + (w * 4 + ni) * 1024 + lane * 16);
#pragma unroll
        for (int j = 0; j < 3; ++j) {
          unsigned p0 = pv[j][ni >> 1][(ni & 1) * 2], p1 = pv[j][ni >> 1][(ni & 1) * 2 + 1];
          v[0] += bf2f(p0 & 0xffffu);
          v[1] += bf2f(p0 >> 16);
          v[2] += bf2f(p1 & 0xffffu);
          v[3] += bf2f(p1 >> 16);
        }
        v[0] += bf2f(xres[ni][0] & 0xffffu);
        v[1] += bf2f(xres[ni][0] >> 16);
        v[2] += bf2f(xres[ni][1] & 0xffffu);
        v[3] += bf2f(xres[ni][1] >> 16);
        *(u32x2*)(out + (size_t)orow * D_ + n0 + wn * 64 + ni * 16 + 4 * g) = (u32x2){pack2(v[0], v[1]), pack2(v[2], v[3])};
      }
    }
    return;
  }

  const int g = lane >> 4, r16 = lane & 15;
  const int aoff = (wm * 64 + r16) * 128 + ((g ^ (r16 & 7)) * 16);
  int boff[4];
  {
    int q = r16 >> 2, p = lane & 3;
    int k = 8 * g + q;
    int f = q | ((g & 1) << 2);
#pragma unroll
    for (int ni = 0; ni < 4; ++ni) {
      int lc = wn * 8 + ni * 2 + (p >> 1);
      boff[ni] = 16384 + k * 256 + ((lc ^ (f << 1)) * 16) + 8 * (p & 1);
    }
  }
  f32x4 acc[4][4];
#pragma unroll
  for (int ni = 0; ni < 4; ++ni)
#pragma unroll
    for (int mi = 0; mi < 4; ++mi) acc[ni][mi] = (f32x4){0.f, 0.f, 0.f, 0.f};

  s16x8 ax[2][4], bx[2][4], ay[2][4], by[2][4];
#define K2_READ(AF, BF, BASE)                                                      \
  do {                                                                             \
    _Pragma("unroll") for (int kk = 0; kk < 2; ++kk)                               \
    _Pragma("unroll") for (int ni = 0; ni < 4; ++ni) {                             \
      AF[kk][ni] = lds_b128((BASE) + (aoff ^ (kk * 64)) + ni * 2048);              \
      s16x4 lo = lds_tr((BASE) + boff[ni] + kk * 8192);                            \
      s16x4 hi = lds_tr((BASE) + boff[ni] + kk * 8192 + 1024);                     \
      BF[kk][ni] = cat(lo, hi);                                                    \
    }                                                                              \
  } while (0)
#define K2_MMA(AF, BF)                                                             \
  do {                                                                             \
    _Pragma("unroll") for (int kk = 0; kk < 2; ++kk)                               \
    _Pragma("unroll") for (int ni = 0; ni < 4; ++ni)                               \
      _Pragma("unroll") for (int mi = 0; mi < 4; ++mi)                             \
        acc[ni][mi] = mfma16(BF[kk][ni], AF[kk][mi], acc[ni][mi]);                 \
  } while (0)
#define K2_SCHED()                                                                 \
  do {                                                                             \
    _Pragma("unroll") for (int q_ = 0; q_ < 24; ++q_) {                            \
      __builtin_amdgcn_sched_group_barrier(0x008, 1, 0);                           \
      __builtin_amdgcn_sched_group_barrier(0x100, 1, 0);                           \
    }                                                                              \
    __builtin_amdgcn_sched_group_barrier(0x008, 8, 0);                             \
  } while (0)

  BARRIER();
  STAMP(dbg, 1);
  K2_READ(ax, bx, smem);
  for (int i = 0; i < K2_NS - 1; i += 2) {
    BARRIER();
    K2_READ(ay, by, smem + ((i + 1) & 3) * K2_STAGE);
    K2_MMA(ax, bx);
    K2_SCHED();
    __builtin_amdgcn_sched_barrier(0);
    BARRIER();
    K2_READ(ax, bx, smem + ((i + 2) & 3) * K2_STAGE);
    K2_MMA(ay, by);
    K2_SCHED();
    __builtin_amdgcn_sched_barrier(0);
  }
  K2_MMA(ax, bx);
  STAMP(dbg, 2);

  __amdgpu_buffer_rsrc_t xr = __builtin_amdgcn_make_buffer_rsrc((void*)xch, 0, XCH_BYTES + FLAG_BYTES, 0x00020000);
#pragma unroll
  for (int mi = 1; mi < 4; ++mi) {
    int dest = (kq + mi) & 3;
    unsigned off = (unsigned)((((tile * 4 + dest) * 3 + (3 - mi)) * 4 + w) * 2048 + lane * 16);
#pragma unroll
    for (int hf = 0; hf < 2; ++hf) {
      u32x4 v;
#pragma unroll
      for (int q = 0; q < 2; ++q) {
        f32x4 a = acc[hf * 2 + q][mi];
        v[q * 2] = pack2(a[0], a[1]);
        v[q * 2 + 1] = pack2(a[2], a[3]);
      }
      __builtin_amdgcn_raw_buffer_store_b128(v, xr, off + hf * 1024, 0, 16);
    }
  }
#pragma unroll
  for (int ni = 0; ni < 4; ++ni) *(LAS f32x4*)(smem + (w * 4 + ni) * 1024 + lane * 16) = acc[ni][0];
  BARRIER();
#define FLAG(mi, n) do { WAIT_V(n); if (lane == 0) __builtin_amdgcn_raw_buffer_store_b32(0x5eedu, xr, XCH_BYTES + (unsigned)(((tile * 4 + ((kq + mi) & 3)) * 4 + w) * 32 + (3 - mi) * 4), 0, 16); } while (0)
  FLAG(1, 4); FLAG(2, 3); FLAG(3, 2);
}

extern "C" void kernel_launch(void* const* d_in, const int* in_sizes, int n_in, void* d_out, int out_size,
                              void* d_ws, size_t ws_size, hipStream_t stream) {
  const u16* X = (const u16*)d_in[0];
  const u16* Cr = (const u16*)d_in[1];
  const u16* Ci = (const u16*)d_in[2];
  const u16* Hr = (const u16*)d_in[3];
  const u16* Hi = (const u16*)d_in[4];
  const u16* W1 = (const u16*)d_in[6];
  const u16* W2 = (const u16*)d_in[7];
  char* ws = (char*)d_ws;
  u16* AB = (u16*)(ws);
  u16* H = (u16*)(ws + 4194304);
  char* xch = ws + 4194304 + 8388608;
  (void)hipFuncSetAttribute((const void*)k1_kernel, hipFuncAttributeMaxDynamicSharedMemorySize, K1_LDS);
  (void)hipFuncSetAttribute((const void*)k2_kernel, hipFuncAttributeMaxDynamicSharedMemorySize, K2_LDS);
  k1_kernel<<<256, 512, K1_LDS, stream>>>(X, Hr, Hi, W1, Cr, Ci, W2, AB, H, xch);
  k2_kernel<<<256, 512, K2_LDS, stream>>>(Cr, Ci, H, AB, W2, X, (u16*)d_out, xch);
}
```

```cpp
#include <hip/hip_runtime.h>
#include <stdint.h>

typedef short s16x8 __attribute__((ext_vector_type(8)));
typedef short s16x4 __attribute__((ext_vector_type(4)));
typedef short s16x2 __attribute__((ext_vector_type(2)));
typedef __bf16 bf16x2 __attribute__((ext_vector_type(2)));
typedef float f32x4 __attribute__((ext_vector_type(4)));
typedef float f32x2 __attribute__((ext_vector_type(2)));
typedef unsigned u32x4 __attribute__((ext_vector_type(4)));
typedef unsigned u32x2 __attribute__((ext_vector_type(2)));
typedef unsigned short u16;
#define LAS __attribute__((address_space(3)))

constexpr int S_ = 2048, D_ = 512, F_ = 1792;

#define WAIT_V(n) asm volatile("s_waitcnt vmcnt(%0)" ::"n"(n) : "memory")
#define WAIT_L0() asm volatile("s_waitcnt lgkmcnt(0)" ::: "memory")
#define BARRIER() do { WAIT_L0(); __builtin_amdgcn_s_barrier(); asm volatile("" ::: "memory"); } while (0)
#define RAWBAR() do { asm volatile("" ::: "memory"); __builtin_amdgcn_s_barrier(); asm volatile("" ::: "memory"); } while (0)
#define TREC()
#define TDECL
#define TEND()
#define CLK(k)

#define P1 0
#define P2 -1
#define PROBE(id) do { if ((id) == P1) CLK(1); if ((id) == P2) CLK(6); } while (0)
constexpr unsigned WS_AB = 0;
constexpr unsigned WS_H = 4u << 20;
constexpr unsigned WS_XCH = 12u << 20;
constexpr unsigned WS_INBOX = 18u << 20;
constexpr unsigned WS_XFLAG = WS_INBOX + 262144;
constexpr unsigned WS_END = WS_XFLAG + 32768;
constexpr unsigned WS_DBG = 28u << 20;
constexpr unsigned SPIN_MAX = 1u << 18;
#define HPRIO 3
constexpr unsigned MAGIC0 = 0x5eedc0deu, MAGIC1 = 0xa11f1a65u;
#define HPS 8
#define HS0 0
#define HPOLL 8
#define CSP0 0
#define CSP1 1
#define CSP2 2
#define CSP3 3
#define SSL -1, -1, 0, 1, 2, 3
#define WSD 4, 4, 5, 6, 6, 7, 8, 8
#define EPRIO 0
#define YB 1
#define EI 0
#define PP 2
#define PFV 2
#define PFN 4
#define PFM 0
#define PFP -1
constexpr int CODE_LINES = 223;
#define BS 2
#define PSLEEP 0

constexpr int SL = 16384;
constexpr int L_R = 8 * SL;
__device__ __forceinline__ constexpr int xs(int s) { return (s < 4 ? 4 + s : s < 6 ? s - 4 : s - 2) * SL; }
__device__ __forceinline__ constexpr int l_h(int s) { return (s < 4 ? 2 * SL : L_R - 4 * 8192) + s * 8192; }
__device__ __forceinline__ constexpr int wsl(int s) { return (s < 4 ? 2 + (s >> 1) + (s & 1) * 4 : s < 6 ? s - 4 : s - 2) * SL; }
__device__ __forceinline__ constexpr int ca(int t) { return ((t & 3) == 0 ? 2 : (t & 3) == 1 ? 6 : (t & 3) == 2 ? 3 : 7) * SL; }
__device__ __forceinline__ constexpr int cb(int t) { return ((t & 3) == 0 ? 0 : (t & 3) == 1 ? 1 : (t & 3) == 2 ? 4 : 5) * SL; }
__device__ __forceinline__ constexpr int ssl(int s) { constexpr int t[6] = {SSL}; return t[s - 2]; }
__device__ __forceinline__ constexpr int wsd(int k) { constexpr int t[8] = {WSD}; return t[k]; }
__device__ __forceinline__ constexpr int issued(int b) {
  int n = 6;
  for (int s = 2; s < 8; ++s) n += ssl(s) <= b ? 6 : 0;
  for (int k = 0; k < 8; ++k) n += wsd(k) <= b ? 4 : 0;
  return n;
}
__device__ __forceinline__ constexpr int endidx(int s) {
  if (s < 2) return 3 * (s + 1);
  int n = issued(ssl(s) - 1);
  for (int q = 2; q <= s; ++q) n += ssl(q) == ssl(s) ? 6 : 0;
  return n;
}
static_assert(ssl(6) >= 1 && ssl(7) >= 2 && wsd(0) >= 2 && wsd(1) >= 3 && wsd(2) >= 4 && wsd(3) >= 4 && wsd(4) >= 5 && wsd(5) >= 6 && wsd(6) >= 7 && wsd(7) >= 8, "slot reuse");
static_assert(ssl(2) < 2 && ssl(3) < 3 && ssl(4) < 4 && ssl(5) < 5 && ssl(6) < 6 && ssl(7) < 7, "stage order");
static_assert(wsd(0) <= wsd(1) && wsd(1) <= wsd(2) && wsd(2) <= wsd(3) && wsd(3) <= wsd(4) && wsd(4) <= wsd(5) && wsd(5) <= wsd(6) && wsd(6) <= wsd(7) && ssl(7) <= wsd(0), "issue order");
constexpr int L_CSA = 15360;
constexpr int L_CSB = L_R + 30720;
constexpr int L_AB = 9 * SL;
constexpr int HP = 240;
constexpr int LDS_BYTES = 163840;
constexpr int C_NS = 23;
#define ROT ((mt + 2 * nt) & 7)

__device__ __forceinline__ float bf2f(unsigned h) { return __uint_as_float(h << 16); }
__device__ __forceinline__ float rbf(float f) {
  __bf16 b = (__bf16)f;
  return (float)b;
}
__device__ __forceinline__ unsigned pack2(float lo, float hi) {
  bf16x2 v;
  v[0] = (__bf16)lo;
  v[1] = (__bf16)hi;
  return __builtin_bit_cast(unsigned, v);
}
__device__ __forceinline__ const char* uni(const char* p) {
  unsigned long long v = (unsigned long long)p;
  unsigned lo = __builtin_amdgcn_readfirstlane((unsigned)v), hi = __builtin_amdgcn_readfirstlane((unsigned)(v >> 32));
  return (const char*)(((unsigned long long)hi << 32) | lo);
}
#define GLDS(voff, sbase, lds, IMM) asm volatile("s_add_u32 m0, %2, %3\n\ts_nop 0\n\tglobal_load_lds_dwordx4 %0, %1" ::"v"(voff), "s"(sbase), "s"(lds), "n"(IMM) : "memory", "scc")
#define GLDC(voff, sbase, lds, IMM) asm volatile("s_add_u32 m0, %2, %3\n\ts_nop 0\n\tglobal_load_lds_dwordx4 %0, %1 sc1" ::"v"(voff), "s"(sbase), "s"(lds), "n"(IMM) : "memory", "scc")
__device__ __forceinline__ int lane_id() {
  int l;
  asm volatile("v_mbcnt_lo_u32_b32 %0, -1, 0\n\tv_mbcnt_hi_u32_b32 %0, -1, %0" : "=v"(l));
  return l;
}
__device__ __forceinline__ s16x8 lds_b128(const char* p) { return *(const LAS s16x8*)p; }
__device__ __forceinline__ s16x4 lds_tr(const char* p) {
  return __builtin_amdgcn_ds_read_tr16_b64_v4i16((LAS s16x4*)p);
}
__device__ __forceinline__ s16x8 cat(s16x4 a, s16x4 b) {
  return __builtin_shufflevector(a, b, 0, 1, 2, 3, 4, 5, 6, 7);
}
__device__ __forceinline__ f32x4 mfma16(s16x8 a, s16x8 b, f32x4 c) {
  return __builtin_amdgcn_mfma_f32_16x16x32_bf16(a, b, c, 0, 0, 0);
}
__device__ __forceinline__ f32x2 unpk(unsigned v) { return (f32x2){__uint_as_float(v << 16), __uint_as_float(v & 0xffff0000u)}; }
#define SGB_MD(n) do { _Pragma("unroll") for (int q_ = 0; q_ < (n); ++q_) { __builtin_amdgcn_sched_group_barrier(0x008, 1, 0); __builtin_amdgcn_sched_group_barrier(0x100, 1, 0); } } while (0)
#define SGB_MDD(n) do { _Pragma("unroll") for (int q_ = 0; q_ < (n); ++q_) { __builtin_amdgcn_sched_group_barrier(0x008, 1, 0); __builtin_amdgcn_sched_group_barrier(0x100, 2, 0); } } while (0)
#define SGB_M(n) __builtin_amdgcn_sched_group_barrier(0x008, (n), 0)

__global__ __launch_bounds__(512, 1) void fused_kernel(const u16* __restrict__ X, const u16* __restrict__ Cr,
                                                       const u16* __restrict__ Ci, const u16* __restrict__ Hr,
                                                       const u16* __restrict__ Hi, const u16* __restrict__ W1,
                                                       const u16* __restrict__ W2, u16* __restrict__ out,
                                                       char* __restrict__ ws) {
  extern __shared__ __attribute__((aligned(1024))) char smem[];
  const int tid = threadIdx.x, lane = tid & 63;
  const int wv = __builtin_amdgcn_readfirstlane(tid >> 6);
  const int w = wv & 3;
  const int wm = w >> 1, wn = w & 1;
  const int bid = blockIdx.x;
  const int idx = bid >> 3;
  unsigned long long pc0;
  asm volatile("s_getpc_b64 %0" : "=s"(pc0));
  if (PFV == 1 ? wv == 0 : PFV == 2 ? (wv < 4 && idx < PFN) : PFV == 3 ? wv < 4 : PFV == 4 ? (wv == 0 || (wv < 4 && idx < PFN)) : false) {
    int l_ = (PFV == 1 || (PFV == 4 && idx >= PFN)) ? idx * 9 + lane : wv * 64 + lane;
    l_ = l_ < CODE_LINES ? l_ : l_ - CODE_LINES;
    const unsigned long long cb_ = pc0 & ~127ull;
    const unsigned ld_ = (unsigned)(size_t)(LAS char*)smem + l_h(7) + wv * 256;
    asm volatile("s_mov_b32 m0, %2\n\ts_nop 0\n\tglobal_load_lds_dword %0, %1 sc1" ::"v"(l_ * 128), "s"(cb_), "s"(ld_) : "memory");
  }
  const int kq = bid & 3, mh = (bid >> 2) & 1;
  const int mt = mh * 8 + (idx >> 2), nt = idx & 3;
  const int j = kq * 4 + nt;
  const int m0 = mt * 128, n0 = nt * 128;
  const int tile = mt * 4 + nt;
  const int rot = ROT;
  __amdgpu_buffer_rsrc_t wr = __builtin_amdgcn_make_buffer_rsrc((void*)ws, 0, WS_END, 0x00020000);
  const u32x2 magic = {MAGIC0, MAGIC1};

  TDECL
  const unsigned lbase = (unsigned)(size_t)(LAS char*)smem;
  const int lch = (lane & 7) ^ ((lane >> 3) & 7);
  const char* hsrc = uni((const char*)(j < 8 ? Hr : Hi));
  {
    const unsigned xo8 = (unsigned)((mt * 128 + wv * 16 + (lane >> 3)) * 1024 + lch * 16);
    const unsigned ho8 = (unsigned)(((j & 7) * 64 + wv * 8 + (lane >> 3)) * 1024 + lch * 16);
    const unsigned xo8b = xo8 + 8192, lx = lbase + wv * 2048, lh = lbase + wv * 1024;
#define FIRST(s) do { const int ko = (((s) + rot) & 7) * 128; const char* xb = (const char*)X + ko; const char* hb = hsrc + ko; \
      if ((s) < 6) { GLDS(xo8, xb, lx, xs(s)); GLDS(xo8b, xb, lx, xs(s) + 1024); }                                                \
      GLDS(ho8, hb, lh, l_h(s)); } while (0)
    FIRST(0); FIRST(1);
  }
  asm volatile("" ::"s"(Cr), "s"(Ci), "s"(W1), "s"(W2), "s"(out), "s"(ws));

  if (wv >= 4) {
    const int pw = w;
    __builtin_amdgcn_s_setprio(HPRIO);
    const int lane = lane_id();
    const int lch = (lane & 7) ^ ((lane >> 3) & 7);
    const unsigned xo = (unsigned)((mt * 128 + pw * 32 + (lane >> 3)) * 1024 + lch * 16);
    const unsigned lx4 = lbase + pw * 4096, lw = lbase + pw * 3840, lh2 = lbase + pw * 2048;
    const unsigned ho = (unsigned)(((j & 7) * 64 + pw * 16 + (lane >> 3)) * 1024 + lch * 16);
#define stageH(s) do { const char* hb = hsrc + (((s) + rot) & 7) * 128; GLDS(ho, hb, lh2, l_h(s)); GLDS(ho + 8192, hb, lh2, l_h(s) + 1024); } while (0)
#define stageX(s) do { const char* xb = (const char*)X + (((s) + rot) & 7) * 128;                                                \
      GLDS(xo, xb, lx4, xs(s)); GLDS(xo + 8192, xb, lx4, xs(s) + 1024); GLDS(xo + 16384, xb, lx4, xs(s) + 2048); GLDS(xo + 24576, xb, lx4, xs(s) + 3072); } while (0)
    float ca4[4] = {0.f, 0.f, 0.f, 0.f};
    const int csrd = (pw * 16 + (lane >> 2)) * 128 + (lane & 3) * 16;
    u32x4 cv0, cv1;
#define CSA_RD(s) do { cv0 = *(const LAS u32x4*)(smem + csrd + l_h(s)); cv1 = *(const LAS u32x4*)(smem + csrd + l_h(s) + 64); } while (0)
#define CSA_ADD() do { const bf16x2 one2 = __builtin_bit_cast(bf16x2, 0x3f803f80u);                                          \
      asm volatile("" : "+v"(cv0), "+v"(cv1));                                                                               \
      _Pragma("unroll") for (int e = 0; e < 4; ++e) { const unsigned u0 = cv0[e], u1 = cv1[e];                               \
        ca4[e] = __builtin_amdgcn_fdot2_f32_bf16(__builtin_bit_cast(bf16x2, u0), one2, ca4[e], false);                       \
        ca4[e] = __builtin_amdgcn_fdot2_f32_bf16(__builtin_bit_cast(bf16x2, u1), one2, ca4[e], false); }                     \
      asm volatile("" : "+v"(ca4[0]), "+v"(ca4[1]), "+v"(ca4[2]), "+v"(ca4[3])); } while (0)
#define DPP_ADD(v, ctl) v += __builtin_bit_cast(float, __builtin_amdgcn_update_dpp(0, __builtin_bit_cast(int, v), ctl, 0xf, 0xf, false))
    const int wrow = lane / 14, wch = lane - wrow * 14;
    const bool wact = lane < 56;
    const unsigned wo = (unsigned)((pw * 16 + wrow) * (F_ * 2) + j * 224 + wch * 16);
    const unsigned wo1 = wo + 4 * F_ * 2, wo2 = wo + 8 * F_ * 2, wo3 = wo + 12 * F_ * 2;
#define stageW(s) do { const char* wsrc = (const char*)W1 + (size_t)(((s) + rot) & 7) * (64 * F_ * 2);                            \
      if (wact) { GLDS(wo, wsrc, lw, wsl(s)); GLDS(wo1, wsrc, lw, wsl(s) + 896); GLDS(wo2, wsrc, lw, wsl(s) + 1920); GLDS(wo3, wsrc, lw, wsl(s) + 2816); } } while (0)
    f32x4 cbs[7];
#pragma unroll
    for (int l = 0; l < 7; ++l) cbs[l] = (f32x4){0.f, 0.f, 0.f, 0.f};
    const int cbrd = (pw * 2 + ((lane >> 4) & 1)) * 1920 + ((lane & 15) >> 2) * 224 + (lane & 3) * 8;
    auto csb = [&](int q) {
      const s16x8 ones = {0x3f80, 0x3f80, 0x3f80, 0x3f80, 0x3f80, 0x3f80, 0x3f80, 0x3f80};
      const char* p = smem + wsl(2 * q) + (lane >> 5) * (wsl(2 * q + 1) - wsl(2 * q)) + cbrd;
      s16x8 f[7];
#pragma unroll
      for (int l = 0; l < 7; ++l) f[l] = cat(lds_tr(p + l * 32), lds_tr(p + l * 32 + 896));
#pragma unroll
      for (int l = 0; l < 7; ++l) cbs[l] = mfma16(ones, f[l], cbs[l]);
      asm volatile("" : "+v"(cbs[0]), "+v"(cbs[1]), "+v"(cbs[2]), "+v"(cbs[3]), "+v"(cbs[4]), "+v"(cbs[5]), "+v"(cbs[6]));
    };
#define CSB(pos, n) do { if (CSP0 == (pos)) { if ((pos) < 3) WAIT_V((n)); csb(0); } if (CSP1 == (pos)) { if ((pos) < 3) WAIT_V((n) - 8); csb(1); } \
      if (CSP2 == (pos)) { if ((pos) < 3) WAIT_V((n) - 16); csb(2); } if (CSP3 == (pos)) { if ((pos) < 3) WAIT_V((n) - 24); csb(3); } } while (0)
    const int rj = (nt + (idx >> 2)) & 3;
    unsigned arow[4], boffg[4];
#pragma unroll
    for (int i = 0; i < 4; ++i) {
      int r = (pw * 4 + i) * 8 + (lane >> 3);
      int grow = m0 + (r & 64) + ((((r >> 4) + kq) & 3) << 4) + (r & 15);
      arow[i] = (unsigned)(grow * 4096 + kq * 1024 + lch * 16);
      int k = (pw * 4 + i) * 4 + (lane >> 4);
      int f = (k & 3) | (((k >> 3) & 1) << 2);
      int lc = (lane & 15) ^ (f << 1);
      boffg[i] = (unsigned)(k * 1024 + n0 * 2 + lc * 16);
    }
    auto kstep = [&](int t) -> int {
      if (t == 0) return 16 + 2 * nt;
      if (t == 1) return rj;
      int s = t + 2 < 21 ? t + 2 : 21;
      s = ((t - 2) & 3) == rj ? s : t - 2;
      return s < 16 + 2 * nt ? s : s + 1;
    };
    auto abase_of = [&](int s) -> const char* { return (s < 8 ? (const char*)Cr : s < 16 ? (const char*)Ci : (const char*)ws + WS_H) + (s & 7) * 128; };
    auto bbase_of = [&](int s) -> const char* {
      return s < 16 ? ws + WS_AB + (size_t)((s & 8) * 256 + kq * 512 + (s & 7) * 64) * 1024 : (const char*)W2 + (size_t)(kq * 448 + (s - 16) * 64) * 1024;
    };
#define CA(t) do { const char* ab_ = uni(abase_of(kstep(t)));                                                                       \
      GLDC(arow[0], ab_, lx4, ca(t)); GLDC(arow[1], ab_, lx4, ca(t) + 1024); GLDC(arow[2], ab_, lx4, ca(t) + 2048); GLDC(arow[3], ab_, lx4, ca(t) + 3072); } while (0)
#define CB(t) do { const char* bb_ = uni(bbase_of(kstep(t)));                                                                       \
      GLDC(boffg[0], bb_, lx4, cb(t)); GLDC(boffg[1], bb_, lx4, cb(t) + 1024); GLDC(boffg[2], bb_, lx4, cb(t) + 2048); GLDC(boffg[3], bb_, lx4, cb(t) + 3072); } while (0)
    const unsigned ab_st = WS_AB + (unsigned)(((j >> 3) * 2048 + mt * 128 + pw * 32 + (lane >> 3)) * 1024 + (j & 7) * 128 + lch * 16);
    const unsigned h_st = wact ? WS_H + (unsigned)((mt * 128 + pw * 32 + wrow) * 4096 + (j >> 2) * 1024 + (j & 3) * 224 + wch * 16) : 0x80000000u;
    u32x4 hv[8];
    int hrd[8];
    auto h_addr = [&]() {
      const int c = wch - 2 * nt;
#pragma unroll
      for (int i = 0; i < 8; ++i) {
        const int R = pw * 32 + i * 4 + (wact ? wrow : 0);
        int own = ca(0) + ((R & 64) + ((((R >> 4) - kq) & 3) << 4) + (R & 15)) * 128 + ((c ^ (R & 7)) * 16), stg = L_R + R * HP + wch * 16;
        asm volatile("" : "+v"(own), "+v"(stg));
        hrd[i] = (unsigned)c < 8u ? own : stg;
        asm volatile("" : "+v"(hrd[i]));
      }
    };
#define H_OUT(i) __builtin_amdgcn_raw_buffer_store_b128(hv[i], wr, h_st, (i) * 4 * 4096, 16)
    const unsigned inbox = WS_INBOX + (unsigned)bid * 1024u;
    const unsigned ab_off = inbox + lane * 8u, h_off = inbox + 512u + (lane & 3) * 8u;
    const unsigned abf = WS_INBOX + (unsigned)((mt >> 2) | (((lane >> 3) & 1) << 2) | ((((lane & 7) << 2) | ((j & 7) >> 1)) << 3)) * 1024u +
                         (unsigned)(((mt & 3) * 4 + (j >> 3) * 2 + (j & 1)) * 4 + pw) * 8u;
    const unsigned hf_off = WS_INBOX + (unsigned)((j >> 2) | ((mt >> 3) << 2) | ((((mt & 7) << 2) | (lane & 3)) << 3)) * 1024u + 512u +
                            (unsigned)(j & 3) * 8u;
    auto spin = [&](unsigned off) {
#pragma unroll 1
      for (unsigned spins = 0; spins < SPIN_MAX; ++spins) {
        asm volatile("" ::: "memory");
        u32x2 f = __builtin_amdgcn_raw_buffer_load_b64(wr, off, 0, 16);
        unsigned long long ok = __builtin_amdgcn_ballot_w64(f[0] == MAGIC0 && f[1] == MAGIC1);
        if (ok == ~0ull) break;
        __builtin_amdgcn_s_sleep(1);
      }
      asm volatile("" ::: "memory");
    };
#define POLL(dst, off) asm volatile("buffer_load_dwordx2 %0, %1, %2, 0 offen sc1" : "=v"(dst) : "v"(off), "s"(wr) : "memory")
#define POLL_OK(dst, n) ({ unsigned f0_, f1_; asm volatile("s_waitcnt vmcnt(%4)\n\tv_mov_b32 %0, %2\n\tv_mov_b32 %1, %3" : "=&v"(f0_), "=&v"(f1_) : "v"(dst[0]), "v"(dst[1]), "n"(n) : "memory"); \
      __builtin_amdgcn_ballot_w64(f0_ == MAGIC0 && f1_ == MAGIC1) == ~0ull; })
    const u32x2 zero2 = {0u, 0u};

#define SLOT(b) do {                                                                                                         \
      if (ssl(2) == (b)) { stageX(2); stageH(2); } if (ssl(3) == (b)) { stageX(3); stageH(3); } if (ssl(4) == (b)) { stageX(4); stageH(4); } \
      if (ssl(5) == (b)) { stageX(5); stageH(5); } if (ssl(6) == (b)) { stageX(6); stageH(6); } if (ssl(7) == (b)) { stageX(7); stageH(7); } \
      if (wsd(0) == (b)) stageW(0); if (wsd(1) == (b)) stageW(1); if (wsd(2) == (b)) stageW(2); if (wsd(3) == (b)) stageW(3); \
      if (wsd(4) == (b)) stageW(4); if (wsd(5) == (b)) stageW(5); if (wsd(6) == (b)) stageW(6); if (wsd(7) == (b)) stageW(7); } while (0)
    SLOT(-1);
    WAIT_V(issued(-1) - endidx(0));
    RAWBAR();
#pragma unroll
    for (int b = 0; b < 7; ++b) {
      if (b < 2) CSA_RD(b);
      SLOT(b);
      CSA_ADD();
      WAIT_V(issued(b) - endidx(b + 1));
      if (b >= 1) CSA_RD(b + 1);
      RAWBAR();
    }
    CSA_ADD();
    {
      float c1 = (ca4[0] + ca4[1]) + (ca4[2] + ca4[3]);
      DPP_ADD(c1, 0xb1);
      DPP_ADD(c1, 0x4e);
      *(LAS unsigned*)(smem + L_CSA + (pw * 16 + (lane >> 2)) * 4) = pack2(c1, c1 - rbf(c1));
    }
    SLOT(7);
    BARRIER();
    SLOT(8);
    WAIT_V(24);
    CSB(0, 24);
    RAWBAR();
    {
      u32x4 v[4];
#pragma unroll
      for (int i = 0; i < 4; ++i) v[i] = *(const LAS u32x4*)(smem + L_AB + (pw * 4 + i) * 1024 + lane * 16);
#pragma unroll
      for (int i = 0; i < 4; ++i) __builtin_amdgcn_raw_buffer_store_b128(v[i], wr, ab_st, i * 8192, 16);
      asm volatile("" ::: "memory");
    }
    CSB(1, 28);
    WAIT_V(20);
    BARRIER();
    CA(1);
    WAIT_V(4);
    if (lane < 16) __builtin_amdgcn_raw_buffer_store_b64(magic, wr, abf, 0, 16);
    CSB(2, 63);
    h_addr();
    BARRIER();
    u32x2 pf0, pf1;
    CA(2); CA(3);
    if (PP == 0) POLL(pf0, ab_off);
    CSB(3, 0);
    if (PP == 1) POLL(pf0, ab_off);
#pragma unroll
    for (int l = 0; l < 7; ++l) {
      float v = cbs[l][0];
      unsigned d = pack2(v, v - rbf(v));
      if (lane < 16) *(LAS unsigned*)(smem + L_CSB + (pw * 112 + l * 16 + lane) * 4) = d;
    }
    if (PP == 2) POLL(pf0, ab_off);
    BARRIER();
    if (PP == 3) POLL(pf0, ab_off);
    CB(0);
    const bool early = POLL_OK(pf0, YB ? 4 : 0);
    if (early) CB(1);
    RAWBAR();
    if (!early) { spin(ab_off); CB(1); }
    CB(2); CB(3);
    if (YB) { WAIT_V(12); RAWBAR(); }
#pragma unroll
    for (int i = 0; i < 8; ++i) hv[i] = *(const LAS u32x4*)(smem + hrd[i]);
    constexpr int HL = HS0 + 8 / HPS - 1;
    constexpr int HFL = HL + 3;
#define EX(t) (((t) == HFL) + ((t) == 2) + ((t) == HPOLL) + ((t) == HPOLL + 4))
#define LD(t) ((t) >= 1 && (t) + 3 < C_NS ? 8 : (t) < 0 ? 4 : 0)
#define HS(t) ((t) >= HS0 && (t) <= HL ? HPS : 0)
#pragma unroll
    for (int t = 0; t < C_NS - 1; ++t) {
      if (t == HPOLL + 2) { if (!POLL_OK(pf1, LD(HPOLL) + HS(HPOLL) + EX(HPOLL + 1) + LD(HPOLL + 1) + HS(HPOLL + 1))) spin(h_off); }
      if (t == HFL) __builtin_amdgcn_raw_buffer_store_b64(magic, wr, pw == 3 && lane < 4 ? hf_off : 0x80000000u, 0, 16);
      if (t == 2) __builtin_amdgcn_raw_buffer_store_b64(zero2, wr, pw == 2 ? ab_off : 0x80000000u, 0, 16);
      if (t == HPOLL + 4) __builtin_amdgcn_raw_buffer_store_b64(zero2, wr, pw == 2 && lane < 4 ? h_off : 0x80000000u, 0, 16);
      if (t == HPOLL) POLL(pf1, h_off);
      asm volatile("" ::: "memory");
      if (LD(t)) { CB(t + 3); CA(t + 3); }
#pragma unroll
      for (int q = 0; q < HPS; ++q) if (HS(t)) H_OUT((t - HS0) * HPS + q);
      asm volatile("" ::: "memory");
      if (t == 0) { WAIT_L0(); WAIT_V(8 + HS(0)); }
      else if (t == 1) WAIT_V(4 + HS(0) + EX(1) + LD(1) + HS(1));
      else WAIT_V((t - 2 == HL ? 0 : HS(t - 2)) + EX(t - 1) + LD(t - 1) + HS(t - 1) + EX(t) + LD(t) + HS(t));
      RAWBAR();
    }
    {
      const int g = lane >> 4, r16 = lane & 15;
      const int orow = m0 + wm * 64 + kq * 16 + r16;
      u32x2 xres[4];
#pragma unroll
      for (int ni = 0; ni < 4; ++ni)
        xres[ni] = *(const u32x2*)(X + (size_t)orow * D_ + n0 + wn * 64 + ni * 16 + 4 * g);
      const unsigned foff = WS_XFLAG + (unsigned)(((tile * 4 + kq) * 4 + w) * 32) + (lane & 3) * 8;
      const unsigned off = WS_XCH + (unsigned)(((tile * 4 + kq) * 3 * 4 + w) * 2048 + lane * 16);
      RAWBAR();
      u32x4 pv[3][2];
      unsigned have = 0;
#pragma unroll
      for (int q = 3; q >= 1; --q) {
#pragma unroll 1
        for (unsigned spins = 0; !(have & (1u << (q - 1))) && spins < SPIN_MAX; ++spins) {
          asm volatile("" ::: "memory");
          u32x2 f = __builtin_amdgcn_raw_buffer_load_b64(wr, foff, 0, 16);
          have = (unsigned)__builtin_amdgcn_ballot_w64(f[0] == MAGIC0 && f[1] == MAGIC1) & 0x7u;
          have = __builtin_amdgcn_readfirstlane(have);
          if (!(have & (1u << (q - 1)))) __builtin_amdgcn_s_sleep(1);
        }
        asm volatile("" ::: "memory");
        pv[q - 1][0] = __builtin_amdgcn_raw_buffer_load_b128(wr, off + (q - 1) * 8192, 0, 16);
        pv[q - 1][1] = __builtin_amdgcn_raw_buffer_load_b128(wr, off + (q - 1) * 8192 + 1024, 0, 16);
      }
      if (lane < 3) __builtin_amdgcn_raw_buffer_store_b64(zero2, wr, foff, 0, 16);
      TREC();
#pragma unroll
      for (int ni = 0; ni < 4; ++ni) {
        f32x4 v = *(const LAS f32x4*)(smem + L_R + (w * 4 + ni) * 1024 + lane * 16);
#pragma unroll
        for (int q = 0; q < 3; ++q) {
          unsigned p0 = pv[q][ni >> 1][(ni & 1) * 2], p1 = pv[q][ni >> 1][(ni & 1) * 2 + 1];
          v[0] += bf2f(p0 & 0xffffu);
          v[1] += bf2f(p0 >> 16);
          v[2] += bf2f(p1 & 0xffffu);
          v[3] += bf2f(p1 >> 16);
        }
        v[0] += bf2f(xres[ni][0] & 0xffffu);
        v[1] += bf2f(xres[ni][0] >> 16);
        v[2] += bf2f(xres[ni][1] & 0xffffu);
        v[3] += bf2f(xres[ni][1] >> 16);
        *(u32x2*)(out + (size_t)orow * D_ + n0 + wn * 64 + ni * 16 + 4 * g) = (u32x2){pack2(v[0], v[1]), pack2(v[2], v[3])};
      }
    }
    return;
  }

  const int g = lane >> 4, r16 = lane & 15;
  const int sw = (g ^ (r16 & 7)) * 16;
  unsigned tv0 = 0;
  float rinv[2];
  unsigned nm2[2];
  s16x8 xf[16][2];
  {
    const int boff = r16 * 128 + sw;
    const int xoff = w * 4096 + boff;
    f32x4 acc[4][2];
#pragma unroll
    for (int ni = 0; ni < 4; ++ni)
#pragma unroll
      for (int mi = 0; mi < 2; ++mi) acc[ni][mi] = (f32x4){0.f, 0.f, 0.f, 0.f};
    f32x4 rs[2], rq[2];
#pragma unroll
    for (int i = 0; i < 2; ++i) { rs[i] = (f32x4){0.f, 0.f, 0.f, 0.f}; rq[i] = (f32x4){0.f, 0.f, 0.f, 0.f}; }
    const s16x8 ones = {0x3f80, 0x3f80, 0x3f80, 0x3f80, 0x3f80, 0x3f80, 0x3f80, 0x3f80};
    s16x8 bf[2][4];
#define A_READ(p) do {                                                                                                      \
    _Pragma("unroll") for (int mi = 0; mi < 2; ++mi)                                                                        \
      xf[p][mi] = lds_b128(smem + xs((p) >> 1) + (xoff ^ (((p) & 1) * 64)) + mi * 2048);                                    \
    _Pragma("unroll") for (int ni = 0; ni < 4; ++ni)                                                                        \
      bf[(p) & 1][ni] = lds_b128(smem + l_h((p) >> 1) + (boff ^ (((p) & 1) * 64)) + ni * 2048); } while (0)
#define WARM() do { if (w < 2 && ((PFM >> w) & 1)) {                                                                        \
      const char* a_;                                                                                                       \
      if (w == 0) { int l_ = idx * 9 + (lane < 18 ? lane : 0); l_ = l_ < CODE_LINES ? l_ : l_ - CODE_LINES; a_ = (const char*)(pc0 & ~127ull) + l_ * 128; } \
      else { const int q_ = lane & 15;                                                                                      \
        a_ = q_ == 0 ? (const char*)Cr + m0 * 4096 : q_ == 1 ? (const char*)Ci + m0 * 4096 : q_ == 2 ? (const char*)W1 : q_ == 3 ? (const char*)W1 + 1835000 - 8 \
           : q_ == 4 ? (const char*)W2 + kq * 448 * 1024 : q_ == 5 ? (const char*)out + m0 * 1024 : q_ < 15 ? ws + (q_ - 6) * (2u << 20) + (q_ < 8 || q_ > 11 ? idx * 4096 : 896) \
           : ws + WS_END - 128; }                                                                                           \
      asm volatile("global_load_dword %0, %1, off sc1" : "+v"(tv0) : "v"(a_) : "memory"); } } while (0)
    if (PFP < 0) WARM();
    if (PFP < 0 && w < 2 && ((PFM >> w) & 1)) WAIT_V(4); else WAIT_V(3);
    BARRIER();
    CLK(0);
    A_READ(0);
    if (PFP == 0) WARM();
    __builtin_amdgcn_sched_barrier(0);
#define STATS(p) do { _Pragma("unroll") for (int mi = 0; mi < 2; ++mi) {                                                    \
      rs[mi] = mfma16(ones, xf[p][mi], rs[mi]); rq[mi] = mfma16(xf[p][mi], xf[p][mi], rq[mi]); } } while (0)
    STATS(0);
    __builtin_amdgcn_sched_barrier(0);
    int ab_wr[4];
#pragma unroll
    for (int p = 0; p < 16; ++p) {
      if (p == 1) { if (PFP <= 0 && w < 2 && ((PFM >> w) & 1)) WAIT_V(1); else WAIT_V(0); }
      if (p == PFP && p > 0) WARM();
      if ((p & 1) && p < 15) BARRIER();
      if (p < 15) A_READ(p + 1);
      else {
#pragma unroll
        for (int ni = 0; ni < 4; ++ni) {
          ab_wr[ni] = L_AB + (w * 32 + r16) * 128 + (g & 1) * 8 + ((ni * 2 + (g >> 1)) ^ (r16 & 7)) * 16;
          asm volatile("" : "+v"(ab_wr[ni]));
        }
      }
#pragma unroll
      for (int ni = 0; ni < 4; ++ni)
#pragma unroll
        for (int mi = 0; mi < 2; ++mi) acc[ni][mi] = mfma16(bf[p & 1][ni], xf[p][mi], acc[ni][mi]);
      if (p < 15) { STATS(p + 1); SGB_MD(6); SGB_M(6); }
      else {
#pragma unroll
        for (int mi = 0; mi < 2; ++mi) {
          float b = (r16 & 2) ? ((r16 & 1) ? rq[mi][3] : rq[mi][2]) : ((r16 & 1) ? rq[mi][1] : rq[mi][0]);
          unsigned bu = g == (r16 >> 2) ? __float_as_uint(b) : 0u;
          auto t1 = __builtin_amdgcn_permlane16_swap(bu, bu, false, false);
          bu = t1[0] | t1[1];
          auto t2 = __builtin_amdgcn_permlane32_swap(bu, bu, false, false);
          b = __uint_as_float(t2[0] | t2[1]);
          float mean = rs[mi][0] * (1.0f / D_);
          float var = b * (1.0f / D_) - mean * mean;
          var = var < 0.f ? 0.f : var;
          rinv[mi] = __builtin_amdgcn_rcpf(rbf(__builtin_amdgcn_sqrtf(rbf(rbf(var) + 1e-5f))));
          nm2[mi] = pack2(-mean, -mean);
          asm volatile("" : "+v"(rinv[mi]), "+v"(nm2[mi]));
        }
#pragma unroll
        for (int q_ = 0; q_ < 8; ++q_) { __builtin_amdgcn_sched_group_barrier(0x008, 1, 0); __builtin_amdgcn_sched_group_barrier(0x002, 6, 0); }
      }
      __builtin_amdgcn_sched_barrier(0);
    }
    PROBE(8);
    if (EPRIO) __builtin_amdgcn_s_setprio(EPRIO);
    BARRIER();
    PROBE(9);
    {
      unsigned cs[4];
#pragma unroll
      for (int ni = 0; ni < 4; ++ni) cs[ni] = *(const LAS unsigned*)(smem + L_CSA + (ni * 16 + r16) * 4);
      __builtin_amdgcn_sched_barrier(0);
#pragma unroll
      for (int ni = 0; ni < 4; ++ni)
#pragma unroll
        for (int mi = 0; mi < 2; ++mi) {
          const unsigned nm = g == 0 ? nm2[mi] : 0u;
          acc[ni][mi] = __builtin_amdgcn_mfma_f32_16x16x16bf16_1k(__builtin_bit_cast(s16x4, (u32x2){cs[ni & 2], cs[(ni & 2) + 1]}),
                                                                  __builtin_bit_cast(s16x4, (ni & 1) ? (u32x2){0u, nm} : (u32x2){nm, 0u}), acc[ni][mi], 0, 0, 0);
        }
      PROBE(11);
      const float sgn = j < 8 ? 1.f : -1.f;
#pragma unroll
      for (int ni = 0; ni < 4; ++ni)
#pragma unroll
        for (int mi = 0; mi < 2; ++mi) {
          const float r = rinv[mi] * sgn;
          const f32x2 lo = (f32x2){acc[ni][mi][0], acc[ni][mi][1]} * (f32x2){r, r}, hi = (f32x2){acc[ni][mi][2], acc[ni][mi][3]} * (f32x2){r, r};
          *(LAS u32x2*)(smem + ab_wr[ni] + mi * 2048) = (u32x2){pack2(lo[0], lo[1]), pack2(hi[0], hi[1])};
        }
    }
    PROBE(0);
    if (EPRIO) __builtin_amdgcn_s_setprio(0);
  }
  {
    const int boff = g * 1920 + (r16 >> 2) * 224 + (lane & 3) * 8;
    f32x4 acc[7][2];
#pragma unroll
    for (int l = 0; l < 7; ++l)
#pragma unroll
      for (int mi = 0; mi < 2; ++mi) acc[l][mi] = (f32x4){0.f, 0.f, 0.f, 0.f};
    s16x8 bf[2][7];
#define B_READ(p) do { const char* wb_ = smem + wsl((p) >> 1) + ((p) & 1) * 7680 + boff;           \
    _Pragma("unroll") for (int l = 0; l < 7; ++l) bf[(p) & 1][l] = cat(lds_tr(wb_ + l * 32), lds_tr(wb_ + l * 32 + 896)); } while (0)
    __builtin_amdgcn_sched_barrier(0);
    asm volatile("s_waitcnt vmcnt(0)" : "+v"(tv0) :: "memory");
    BARRIER();
    CLK(2);
    B_READ(0);
    __builtin_amdgcn_sched_barrier(0);
    unsigned cs[8];
    int h_wr[7][2], st[2], ow[2];
#pragma unroll
    for (int p = 0; p < 16; ++p) {
      if ((p & 3) == 3 && (p < 15 || YB)) BARRIER();
      if (p == 11) PROBE(15);
      if (p == 6) {
#pragma unroll
        for (int mi = 0; mi < 2; ++mi) {
          st[mi] = L_R + (w * 32 + mi * 16 + r16) * HP + g * 8;
          ow[mi] = ca(0) + (wm * 64 + r16) * 128 + (g & 1) * 8 + ((2 * w + mi - kq) & 3) * 2048 + (((g >> 1) ^ (r16 & 7)) * 16);
          asm volatile("" : "+v"(st[mi]), "+v"(ow[mi]));
        }
      }
      if (p >= 8 && p < 15) {
        const int l = p - 8;
#pragma unroll
        for (int mi = 0; mi < 2; ++mi) {
          int o = (ow[mi] ^ (((l - nt) & 3) << 5)) - l * 32;
          asm volatile("" : "+v"(o));
          h_wr[l][mi] = (unsigned)(l - nt) < 4u ? o : st[mi];
          asm volatile("" : "+v"(h_wr[l][mi]));
        }
      }
      if (p < 15) B_READ(p + 1);
      else {
#pragma unroll
        for (int l = 0; l < 8; ++l) cs[l] = *(const LAS unsigned*)(smem + L_CSB + (g * 112 + (l < 7 ? l : 5) * 16 + r16) * 4);
      }
#pragma unroll
      for (int l = 0; l < 7; ++l)
#pragma unroll
        for (int mi = 0; mi < 2; ++mi) acc[l][mi] = mfma16(bf[p & 1][l], xf[p][mi], acc[l][mi]);
      if (p == 15) { SGB_MD(5); SGB_M(9); }
      else if (BS == 1 || (BS == 2 && (p & 3) == 2)) { SGB_MDD(7); SGB_M(7); }
      else SGB_MD(14);
      __builtin_amdgcn_sched_barrier(0);
    }
    PROBE(12);
    if (EPRIO) __builtin_amdgcn_s_setprio(EPRIO);
#pragma unroll
    for (int l = 0; l < 7; ++l)
#pragma unroll
      for (int mi = 0; mi < 2; ++mi)
        acc[l][mi] = __builtin_amdgcn_mfma_f32_16x16x16bf16_1k(__builtin_bit_cast(s16x4, (u32x2){cs[l & 6], cs[(l & 6) + 1]}),
                                                               __builtin_bit_cast(s16x4, (l & 1) ? (u32x2){0u, nm2[mi]} : (u32x2){nm2[mi], 0u}), acc[l][mi], 0, 0, 0);
    CLK(3);
    unsigned pk[7][2][2];
#pragma unroll
    for (int l = 0; l < 7; ++l)
#pragma unroll
      for (int mi = 0; mi < 2; ++mi) {
        const s16x2 z = {0, 0};
        const f32x2 r2 = {rinv[mi], rinv[mi]};
        f32x2 lo = (f32x2){acc[l][mi][0], acc[l][mi][1]} * r2, hi = (f32x2){acc[l][mi][2], acc[l][mi][3]} * r2;
        unsigned p0 = pack2(lo[0], lo[1]), p1 = pack2(hi[0], hi[1]);
        asm("" : "+v"(p0), "+v"(p1));
        pk[l][mi][0] = __builtin_bit_cast(unsigned, __builtin_elementwise_max(__builtin_bit_cast(s16x2, p0), z));
        pk[l][mi][1] = __builtin_bit_cast(unsigned, __builtin_elementwise_max(__builtin_bit_cast(s16x2, p1), z));
        *(LAS u32x2*)(smem + h_wr[l][mi] + l * 32) = (u32x2){pk[l][mi][0], pk[l][mi][1]};
      }
    if (EI) {
      SGB_M(4);
#pragma unroll
      for (int q_ = 0; q_ < 5; ++q_) { SGB_M(2); __builtin_amdgcn_sched_group_barrier(0x002, 12, 0); __builtin_amdgcn_sched_group_barrier(0x200, 2, 0); }
#pragma unroll
      for (int q_ = 0; q_ < 2; ++q_) { __builtin_amdgcn_sched_group_barrier(0x002, 12, 0); __builtin_amdgcn_sched_group_barrier(0x200, 2, 0); }
      __builtin_amdgcn_sched_barrier(0);
    }
    PROBE(13);
  }
  const int aoff = (wm * 64 + r16) * 128 + sw;
  int boff[4];
  {
    int q = r16 >> 2, p = lane & 3;
    int k = 8 * g + q;
    int f = q | ((g & 1) << 2);
#pragma unroll
    for (int ni = 0; ni < 4; ++ni) {
      int lc = wn * 8 + ni * 2 + (p >> 1);
      boff[ni] = k * 256 + ((lc ^ (f << 1)) * 16) + 8 * (p & 1);
    }
  }
  f32x4 acc[4][4];
#pragma unroll
  for (int ni = 0; ni < 4; ++ni)
#pragma unroll
    for (int mi = 0; mi < 4; ++mi) acc[ni][mi] = (f32x4){0.f, 0.f, 0.f, 0.f};
  s16x8 ax[2][4], bx[2][4], ay[2][4], by[2][4];
#define C_READ(AF, BF, ABASE, BBASE)                                               \
  do {                                                                             \
    _Pragma("unroll") for (int kk = 0; kk < 2; ++kk)                               \
    _Pragma("unroll") for (int ni = 0; ni < 4; ++ni) {                             \
      AF[kk][ni] = lds_b128((ABASE) + (aoff ^ (kk * 64)) + ni * 2048);             \
      s16x4 lo = lds_tr((BBASE) + boff[ni] + kk * 8192);                           \
      s16x4 hi = lds_tr((BBASE) + boff[ni] + kk * 8192 + 1024);                    \
      BF[kk][ni] = cat(lo, hi);                                                    \
    }                                                                              \
  } while (0)
#define C_MMA(AF, BF)                                                              \
  do {                                                                             \
    _Pragma("unroll") for (int kk = 0; kk < 2; ++kk)                               \
    _Pragma("unroll") for (int ni = 0; ni < 4; ++ni)                               \
      _Pragma("unroll") for (int mi = 0; mi < 4; ++mi)                             \
        acc[ni][mi] = mfma16(BF[kk][ni], AF[kk][mi], acc[ni][mi]);                 \
  } while (0)
#define C_SCHED() do { SGB_MD(24); SGB_M(8); __builtin_amdgcn_sched_barrier(0); } while (0)
#define C_STEP(NA, NB, OA, OB, ABASE, BBASE) do { C_READ(NA, NB, ABASE, BBASE); C_MMA(OA, OB); C_SCHED(); } while (0)
  PROBE(14);
  if (EPRIO) __builtin_amdgcn_s_setprio(0);
  __builtin_amdgcn_sched_barrier(0);
  BARRIER();
  CLK(4);
  C_READ(ax, bx, smem + ca(0), smem + cb(0));
#pragma unroll 1
  for (int n = 0; n < 5; ++n) {
    BARRIER();
    C_STEP(ay, by, ax, bx, smem + ca(1), smem + cb(1));
    BARRIER();
    C_STEP(ax, bx, ay, by, smem + ca(2), smem + cb(2));
    BARRIER();
    C_STEP(ay, by, ax, bx, smem + ca(3), smem + cb(3));
    BARRIER();
    C_STEP(ax, bx, ay, by, smem + ca(0), smem + cb(0));
  }
  BARRIER();
  C_STEP(ay, by, ax, bx, smem + ca(1), smem + cb(1));
  BARRIER();
  C_STEP(ax, bx, ay, by, smem + ca(2), smem + cb(2));
#define C_MMA1(mi) do { _Pragma("unroll") for (int kk = 0; kk < 2; ++kk) _Pragma("unroll") for (int ni = 0; ni < 4; ++ni)   \
      acc[ni][mi] = mfma16(bx[kk][ni], ax[kk][mi], acc[ni][mi]); } while (0)
#define SEND(mi) do { const unsigned off = WS_XCH + (unsigned)((((tile * 4 + ((kq + mi) & 3)) * 3 + (3 - mi)) * 4 + w) * 2048 + lane * 16); \
      _Pragma("unroll") for (int hf = 0; hf < 2; ++hf) { u32x4 v;                                                           \
        _Pragma("unroll") for (int q = 0; q < 2; ++q) { f32x4 a = acc[hf * 2 + q][mi]; v[q * 2] = pack2(a[0], a[1]); v[q * 2 + 1] = pack2(a[2], a[3]); } \
        __builtin_amdgcn_raw_buffer_store_b128(v, wr, off + hf * 1024, 0, 16); } } while (0)
#define SGB_SEND() do { SGB_M(2); _Pragma("unroll") for (int q_ = 0; q_ < 2; ++q_) {                                        \
      __builtin_amdgcn_sched_group_barrier(0x008, 1, 0); __builtin_amdgcn_sched_group_barrier(0x002, 2, 0);                 \
      __builtin_amdgcn_sched_group_barrier(0x008, 1, 0); __builtin_amdgcn_sched_group_barrier(0x002, 2, 0);                 \
      __builtin_amdgcn_sched_group_barrier(0x008, 1, 0); __builtin_amdgcn_sched_group_barrier(0x040, 1, 0); }               \
      __builtin_amdgcn_sched_barrier(0); } while (0)
  C_MMA1(1);
  __builtin_amdgcn_sched_barrier(0);
  C_MMA1(2); SEND(1); SGB_SEND();
  C_MMA1(3); SEND(2); SGB_SEND();
  C_MMA1(0); SEND(3); SGB_SEND();
  CLK(5);
#pragma unroll
  for (int ni = 0; ni < 4; ++ni) *(LAS f32x4*)(smem + L_R + (w * 4 + ni) * 1024 + lane * 16) = acc[ni][0];
  BARRIER();
  {
#define FLAG(mi, n) do { WAIT_V(n); if (lane == 0) __builtin_amdgcn_raw_buffer_store_b64(magic, wr, WS_XFLAG + (unsigned)(((tile * 4 + ((kq + mi) & 3)) * 4 + w) * 32 + (3 - mi) * 8), 0, 16); } while (0)
    FLAG(1, 4); FLAG(2, 3); FLAG(3, 2);
  }
  TEND();
}

extern "C" void kernel_launch(void* const* d_in, const int* in_sizes, int n_in, void* d_out, int out_size,
                              void* d_ws, size_t ws_size, hipStream_t stream) {
  (void)hipFuncSetAttribute((const void*)fused_kernel, hipFuncAttributeMaxDynamicSharedMemorySize, LDS_BYTES);
  fused_kernel<<<256, 512, LDS_BYTES, stream>>>((const u16*)d_in[0], (const u16*)d_in[1], (const u16*)d_in[2],
                                                (const u16*)d_in[3], (const u16*)d_in[4], (const u16*)d_in[6],
                                                (const u16*)d_in[7], (u16*)d_out, (char*)d_ws);
}
```
